```python
import torch

def _softmax(x, axis=-1):
    return torch.softmax(x, dim=axis)

def _gelu(x):
    return torch.nn.functional.gelu(x, approximate='tanh')

def _silu(x):
    return torch.nn.functional.silu(x)

def _sigmoid(x):
    return torch.sigmoid(x)

def _layernorm(x, axis=-1, eps=1e-05):
    m = x.mean(dim=axis, keepdim=True)
    v = x.var(dim=axis, keepdim=True, unbiased=False)
    return (x - m) / torch.sqrt(v + eps)

def _scatter_add(tgt, idx, src, axis=0):
    return tgt.clone().index_add_(axis, idx, src)

def _segment_sum(src, seg, n):
    out = torch.zeros((n,) + src.shape[1:], dtype=src.dtype)
    return out.index_add_(0, seg, src)

def _pad(x, pw):
    flat = tuple((v for pair in reversed(pw) for v in pair))
    return torch.nn.functional.pad(x, flat)
kernel_name = 'adaLN_cond__sliding_local_dilated__mixer_channel_mlp__prompt'

def setup_inputs():

    _w = lambda a: (lambda t: t.to(torch.bfloat16) if t.is_floating_point() else t)(torch.as_tensor(a))

    def _A_setup():
        B, S, D = (1, 2048, 512)
        return {'x': _w((lambda *s: torch.randn(*s, dtype=torch.bfloat16) * 0.05)(B, S, D)), 'wqkv': _w((lambda *s: torch.randn(*s, dtype=torch.bfloat16) * 0.05)(D, 3 * D)), 'wo': _w((lambda *s: torch.randn(*s, dtype=torch.bfloat16) * 0.05)(D, D))}
    _A_in = _A_setup()

    def _B_setup():
        B, S, D, F = (1, 2048, 512, 1376)
        return {'x': _w((lambda *s: torch.randn(*s, dtype=torch.bfloat16) * 0.05)(B, S, D)), 'w1': _w((lambda *s: torch.randn(*s, dtype=torch.bfloat16) * 3.2)(D, F) * D ** (-0.5)), 'b1': _w((lambda *s: torch.randn(*s, dtype=torch.bfloat16) * 1.6)(F)), 'w2': _w((lambda *s: torch.randn(*s, dtype=torch.bfloat16) * 1.6)(F, D) * F ** (-0.5)), 'b2': _w((lambda *s: torch.randn(*s, dtype=torch.bfloat16) * 1.6)(D))}
    _B_in = _B_setup()
    return {'A_x': _A_in['x'], 'A_wqkv': _A_in['wqkv'], 'A_wo': _A_in['wo'], 'B_x': _B_in['x'], 'B_w1': _B_in['w1'], 'B_b1': _B_in['b1'], 'B_w2': _B_in['w2'], 'B_b2': _B_in['b2'], 'c': _w((lambda *s: torch.randn(*s, dtype=torch.bfloat16) * 0.2)(1, 512) * 8.0), 'w_mod': _w((lambda *s: torch.randn(*s, dtype=torch.bfloat16) * 0.2)(512, 6 * 512) * 512 ** (-0.5) * 8.0)}

def reference(A_x, A_wqkv, A_wo, B_x, B_w1, B_b1, B_w2, B_b2, c, w_mod):

    def _A_ref(x, wqkv, wo):
        B, S, D = x.shape
        H = 32
        Dh = D // H
        W, dil = (8, 2)
        qkv = (lambda x, *d: x.permute(*d))((x @ wqkv).reshape(B, S, 3, H, Dh), 2, 0, 3, 1, 4)
        q, k, v = (qkv[0], qkv[1], qkv[2])
        scale = Dh ** (-0.5)
        pos = torch.arange(S)
        off = torch.arange(W) * dil
        idx = pos[:, None] - off[None, :]
        mask = idx >= 0
        idx_c = torch.where(mask, idx, 0).reshape(-1)
        kg = k[:, :, idx_c, :].reshape(B, H, S, W, Dh)
        vg = v[:, :, idx_c, :].reshape(B, H, S, W, Dh)
        logits = torch.einsum('bhsd,bhswd->bhsw', q, kg) * scale
        logits = torch.where(mask[None, None, :, :], logits, -1000000000.0)
        attn = _softmax(logits, axis=-1)
        o = torch.einsum('bhsw,bhswd->bhsd', attn, vg)
        return (lambda x, *d: x.permute(*d))(o, 0, 2, 1, 3).reshape(B, S, D) @ wo

    def _B_ref(x, w1, b1, w2, b2):
        y = _layernorm(x, axis=-1)
        h = _gelu(torch.einsum('bsd,df->bsf', y, w1) + b1)
        return torch.einsum('bsf,fd->bsd', h, w2) + b2
    x = A_x
    mod = (_silu(c) @ w_mod).reshape(c.shape[0], 6, 1, -1)
    sa, ca, ga, sb, cb, gb = (mod[:, 0].clone(), mod[:, 1].clone(), mod[:, 2].clone(), mod[:, 3].clone(), mod[:, 4].clone(), mod[:, 5].clone())
    h = _layernorm(x, axis=-1) * (1 + ca) + sa
    x = x + ga * _A_ref(h, A_wqkv, A_wo)
    h = _layernorm(x, axis=-1) * (1 + cb) + sb
    x = x + gb * _B_ref(h, B_w1, B_b1, B_w2, B_b2)
    return x
```

```cpp
#include <hip/hip_runtime.h>
#include <hip/hip_bf16.h>
typedef unsigned short u16;
typedef __attribute__((ext_vector_type(8))) short bf16x8;
typedef __attribute__((ext_vector_type(4))) float f32x4;
__device__ __forceinline__ float b2f(u16 v){ return __uint_as_float(((unsigned)v)<<16); }
__device__ __forceinline__ u16 f2b(float f){ __hip_bfloat16 h=__float2bfloat16(f); return *(u16*)&h; }

__global__ void k_mod(const u16* c,const u16* wm,float* mod){
  int j=blockIdx.x*64+threadIdx.x; float a=0;
  for(int k=0;k<512;k++){ float cv=b2f(c[k]); float s=cv/(1.f+__expf(-cv)); s=b2f(f2b(s)); a+=s*b2f(wm[k*3072+j]); }
  mod[j]=b2f(f2b(a));
}
__device__ float wsum(float v){ for(int o=32;o;o>>=1) v+=__shfl_xor(v,o); return v; }
__global__ void k_ln(const u16* x,const float* sh,const float* sc,u16* out,int twice){
  int r=blockIdx.x, l=threadIdx.x; float v[8]; float s=0;
  for(int i=0;i<8;i++){ v[i]=b2f(x[r*512+l*8+i]); s+=v[i]; }
  float m=wsum(s)/512; float q=0; for(int i=0;i<8;i++){ v[i]-=m; q+=v[i]*v[i]; }
  float rs=rsqrtf(wsum(q)/512+1e-5f);
  for(int i=0;i<8;i++){ v[i]=v[i]*rs*(1.f+sc[l*8+i])+sh[l*8+i]; }
  if(twice){ s=0; for(int i=0;i<8;i++) s+=v[i]; m=wsum(s)/512; q=0; for(int i=0;i<8;i++){ v[i]-=m; q+=v[i]*v[i]; }
    rs=rsqrtf(wsum(q)/512+1e-5f); for(int i=0;i<8;i++) v[i]*=rs; }
  for(int i=0;i<8;i++) out[r*512+l*8+i]=f2b(v[i]);
}
__global__ void k_gemm(const u16* A,const u16* B,u16* C,int N,int K,int mode,const u16* bias,const float* g,const u16* res){
  int l=threadIdx.x; int n0=blockIdx.x*16, m0=blockIdx.y*16; f32x4 acc={0,0,0,0};
  for(int k0=0;k0<K;k0+=32){ bf16x8 a,b; int kk=k0+(l>>4)*8;
    a=*(const bf16x8*)(A+(m0+(l&15))*K+kk);
    for(int j=0;j<8;j++) b[j]=B[(kk+j)*N+n0+(l&15)];
    acc=__builtin_amdgcn_mfma_f32_16x16x32_bf16(a,b,acc,0,0,0);
  }
  for(int j=0;j<4;j++){ int m=m0+(l>>4)*4+j,n=n0+(l&15); float v=acc[j];
    if(mode==2||mode==3) v+=b2f(bias[n]);
    if(mode==2){ float t=0.7978845608f*(v+0.044715f*v*v*v); v=0.5f*v*(1.f+tanhf(t)); }
    if(mode==1||mode==3) v=b2f(res[m*N+n])+g[n]*v;
    C[m*N+n]=f2b(v);
  }
}
__global__ void k_attn(const u16* qkv,u16* o){
  int t=blockIdx.x*blockDim.x+threadIdx.x; int s=t>>5,h=t&31; float q[16];
  for(int d=0;d<16;d++) q[d]=b2f(qkv[s*1536+h*16+d]);
  float lg[8],mx=-1e30f;
  for(int w=0;w<8;w++){ int p=s-2*w; if(p<0){lg[w]=-1e9f;} else { float a=0; for(int d=0;d<16;d++) a+=q[d]*b2f(qkv[p*1536+512+h*16+d]); lg[w]=a*0.25f;} mx=fmaxf(mx,lg[w]); }
  float sum=0; for(int w=0;w<8;w++){ lg[w]=__expf(lg[w]-mx); sum+=lg[w]; }
  float acc[16]; for(int d=0;d<16;d++) acc[d]=0;
  for(int w=0;w<8;w++){ int p=s-2*w; if(p<0) p=0; for(int d=0;d<16;d++) acc[d]+=lg[w]*b2f(qkv[p*1536+1024+h*16+d]); }
  for(int d=0;d<16;d++) o[s*512+h*16+d]=f2b(acc[d]/sum);
}
extern "C" void kernel_launch(void* const* d_in,const int* in_sizes,int n_in,void* d_out,int out_size,void* d_ws,size_t ws_size,hipStream_t stream){
  const u16* x=(const u16*)d_in[0]; const u16* wqkv=(const u16*)d_in[1]; const u16* wo=(const u16*)d_in[2];
  const u16* w1=(const u16*)d_in[4]; const u16* b1=(const u16*)d_in[5]; const u16* w2=(const u16*)d_in[6]; const u16* b2=(const u16*)d_in[7];
  const u16* c=(const u16*)d_in[8]; const u16* wm=(const u16*)d_in[9];
  char* ws=(char*)d_ws; float* mod=(float*)ws; u16* h=(u16*)(ws+(1<<20)); u16* qkv=(u16*)(ws+(4<<20)); u16* o=(u16*)(ws+(12<<20));
  u16* x1=(u16*)(ws+(16<<20)); u16* y=(u16*)(ws+(20<<20)); u16* h1=(u16*)(ws+(24<<20));
  k_mod<<<48,64,0,stream>>>(c,wm,mod);
  k_ln<<<2048,64,0,stream>>>(x,mod,mod+512,h,0);
  k_gemm<<<dim3(96,128),64,0,stream>>>(h,wqkv,qkv,1536,512,0,nullptr,nullptr,nullptr);
  k_attn<<<256,256,0,stream>>>(qkv,o);
  k_gemm<<<dim3(32,128),64,0,stream>>>(o,wo,x1,512,512,1,nullptr,mod+1024,x);
  k_ln<<<2048,64,0,stream>>>(x1,mod+1536,mod+2048,y,1);
  k_gemm<<<dim3(86,128),64,0,stream>>>(y,w1,h1,1376,512,2,b1,nullptr,nullptr);
  k_gemm<<<dim3(32,128),64,0,stream>>>(h1,w2,(u16*)d_out,512,1376,3,b2,mod+2560,x1);
}
```

```cpp
#include <hip/hip_runtime.h>
#include <hip/hip_bf16.h>
typedef unsigned short u16;
typedef __attribute__((ext_vector_type(8))) short bf16x8;
typedef __attribute__((ext_vector_type(4))) float f32x4;
__device__ __forceinline__ float b2f(u16 v){ return __uint_as_float(((unsigned)v)<<16); }
__device__ __forceinline__ u16 f2b(float f){ __hip_bfloat16 h=__float2bfloat16(f); return *(u16*)&h; }

__global__ void k_mod(const u16* c,const u16* wm,float* mod){
  int j=blockIdx.x*64+threadIdx.x; float a=0;
  for(int k=0;k<512;k++){ float cv=b2f(c[k]); float s=cv/(1.f+__expf(-cv)); s=b2f(f2b(s)); a+=s*b2f(wm[k*3072+j]); }
  mod[j]=b2f(f2b(a));
}
__device__ float wsum(float v){ for(int o=32;o;o>>=1) v+=__shfl_xor(v,o); return v; }
__global__ void k_ln(const u16* x,const float* sh,const float* sc,u16* out,int twice){
  int r=blockIdx.x, l=threadIdx.x; float v[8]; float s=0;
  for(int i=0;i<8;i++){ v[i]=b2f(x[r*512+l*8+i]); s+=v[i]; }
  float m=wsum(s)/512; float q=0; for(int i=0;i<8;i++){ v[i]-=m; q+=v[i]*v[i]; }
  float rs=rsqrtf(wsum(q)/512+1e-5f);
  for(int i=0;i<8;i++){ v[i]=v[i]*rs*(1.f+sc[l*8+i])+sh[l*8+i]; }
  if(twice){ s=0; for(int i=0;i<8;i++) s+=v[i]; m=wsum(s)/512; q=0; for(int i=0;i<8;i++){ v[i]-=m; q+=v[i]*v[i]; }
    rs=rsqrtf(wsum(q)/512+1e-5f); for(int i=0;i<8;i++) v[i]*=rs; }
  for(int i=0;i<8;i++) out[r*512+l*8+i]=f2b(v[i]);
}
__global__ void k_gemm(const u16* A,const u16* B,u16* C,int N,int K,int mode,const u16* bias,const float* g,const u16* res){
  int l=threadIdx.x; int n0=blockIdx.x*16, m0=blockIdx.y*16; f32x4 acc={0,0,0,0};
  for(int k0=0;k0<K;k0+=32){ bf16x8 a,b; int kk=k0+(l>>4)*8;
    a=*(const bf16x8*)(A+(m0+(l&15))*K+kk);
    for(int j=0;j<8;j++) b[j]=B[(kk+j)*N+n0+(l&15)];
    acc=__builtin_amdgcn_mfma_f32_16x16x32_bf16(a,b,acc,0,0,0);
  }
  for(int j=0;j<4;j++){ int m=m0+(l>>4)*4+j,n=n0+(l&15); float v=acc[j];
    if(mode==2||mode==3) v+=b2f(bias[n]);
    if(mode==2){ float t=0.7978845608f*(v+0.044715f*v*v*v); v=0.5f*v*(1.f+tanhf(t)); }
    if(mode==1||mode==3) v=b2f(res[m*N+n])+g[n]*v;
    C[m*N+n]=f2b(v);
  }
}
__global__ void k_attn(const u16* qkv,u16* o){
  int t=blockIdx.x*blockDim.x+threadIdx.x; int s=t>>5,h=t&31; float q[16];
  for(int d=0;d<16;d++) q[d]=b2f(qkv[s*1536+h*16+d]);
  float lg[8],mx=-1e30f;
  for(int w=0;w<8;w++){ int p=s-2*w; if(p<0){lg[w]=-1e9f;} else { float a=0; for(int d=0;d<16;d++) a+=q[d]*b2f(qkv[p*1536+512+h*16+d]); lg[w]=a*0.25f;} mx=fmaxf(mx,lg[w]); }
  float sum=0; for(int w=0;w<8;w++){ lg[w]=__expf(lg[w]-mx); sum+=lg[w]; }
  float acc[16]; for(int d=0;d<16;d++) acc[d]=0;
  for(int w=0;w<8;w++){ int p=s-2*w; if(p<0) p=0; for(int d=0;d<16;d++) acc[d]+=lg[w]*b2f(qkv[p*1536+1024+h*16+d]); }
  for(int d=0;d<16;d++) o[s*512+h*16+d]=f2b(acc[d]/sum);
}

#define R4(x) x x x x
#define R16(x) R4(R4(x))
#define R64(x) R4(R16(x))
#define R256(x) R4(R64(x))
#define R1024(x) R4(R256(x))
__global__ void k_pre(unsigned* ws){ if(threadIdx.x==0) ws[(115<<20)/4]=(unsigned)__builtin_amdgcn_s_memrealtime(); }
__global__ void k_post(unsigned* ws,int K){
  unsigned tp=ws[(115<<20)/4];
  unsigned mn=0xffffffffu;
  for(int i=threadIdx.x;i<256;i+=64){ unsigned e=ws[(116<<20)/4+i]-tp; mn=min(mn,e); }
  for(int o=32;o;o>>=1){ mn=min(mn,(unsigned)__shfl_xor((int)mn,o)); }
  unsigned v=min(mn*K,100000u);
  unsigned t1=(unsigned)__builtin_amdgcn_s_memrealtime();
  while((unsigned)__builtin_amdgcn_s_memrealtime()-t1<v){}
}
__global__ void k_t(unsigned* ws){ unsigned t=(unsigned)__builtin_amdgcn_s_memrealtime(); if(threadIdx.x==0) ws[(116<<20)/4+blockIdx.x]=t; }
extern "C" void kernel_launch(void* const* d_in,const int* in_sizes,int n_in,void* d_out,int out_size,void* d_ws,size_t ws_size,hipStream_t stream){
  const u16* x=(const u16*)d_in[0]; const u16* wqkv=(const u16*)d_in[1]; const u16* wo=(const u16*)d_in[2];
  const u16* w1=(const u16*)d_in[4]; const u16* b1=(const u16*)d_in[5]; const u16* w2=(const u16*)d_in[6]; const u16* b2=(const u16*)d_in[7];
  const u16* c=(const u16*)d_in[8]; const u16* wm=(const u16*)d_in[9];
  char* ws=(char*)d_ws; float* mod=(float*)ws; u16* h=(u16*)(ws+(1<<20)); u16* qkv=(u16*)(ws+(4<<20)); u16* o=(u16*)(ws+(12<<20));
  u16* x1=(u16*)(ws+(16<<20)); u16* y=(u16*)(ws+(20<<20)); u16* h1=(u16*)(ws+(24<<20));
  k_pre<<<1,64,0,stream>>>((unsigned*)ws);
  k_t<<<256,256,0,stream>>>((unsigned*)ws);
  k_post<<<1,64,0,stream>>>((unsigned*)ws,0);
  k_mod<<<48,64,0,stream>>>(c,wm,mod);
  k_ln<<<2048,64,0,stream>>>(x,mod,mod+512,h,0);
  k_gemm<<<dim3(96,128),64,0,stream>>>(h,wqkv,qkv,1536,512,0,nullptr,nullptr,nullptr);
  k_attn<<<256,256,0,stream>>>(qkv,o);
  k_gemm<<<dim3(32,128),64,0,stream>>>(o,wo,x1,512,512,1,nullptr,mod+1024,x);
  k_ln<<<2048,64,0,stream>>>(x1,mod+1536,mod+2048,y,1);
  k_gemm<<<dim3(86,128),64,0,stream>>>(y,w1,h1,1376,512,2,b1,nullptr,nullptr);
  k_gemm<<<dim3(32,128),64,0,stream>>>(h1,w2,(u16*)d_out,512,1376,3,b2,mod+2560,x1);
}
```

```cpp
#include <hip/hip_runtime.h>

#define D_(n) const void* n, unsigned n##_bytes, unsigned n##_fmt
__global__ __launch_bounds__(256, 1) void adaln_fused(D_(x), D_(wqkv), D_(wo), D_(w1), D_(w2), D_(wmod), D_(ws), D_(out), D_(b1), D_(b2), D_(c)) {
#if defined(__HIP_DEVICE_COMPILE__) && __HIP_DEVICE_COMPILE__
  unsigned tid = __builtin_amdgcn_workitem_id_x();
  asm volatile(
    "L_start:\n"
    "s_getpc_b64 s[4:5]\n"
    "s_load_dwordx16 s[32:47], s[0:1], 64\n"
    "s_load_dwordx16 s[16:31], s[0:1], 0\n"
    "s_load_dwordx4 s[56:59], s[0:1], 160\n"
    "s_load_dwordx8 s[48:55], s[0:1], 128\n"
    "v_mov_b32 v16, v0\n"
    "v_lshrrev_b32 v17, 6, v0\n"
    "v_and_b32 v0, 63, v0\n"
    "v_readfirstlane_b32 s6, v17\n"
    "s_and_b32 s60, s2, 7\n"
    "s_lshr_b32 s61, s2, 3\n"
    "s_lshl_b32 s7, s61, 2\n"
    "s_add_u32 s7, s7, s6\n"
    "s_lshl_b32 s7, s7, 9\n"
    "v_and_b32 v18, 3, v0\n"
    "v_lshlrev_b32 v18, 7, v18\n"
    "v_add_u32 v18, s7, v18\n"
    "v_min_u32 v18, L_end-L_start-8, v18\n"
    "global_load_dword v19, v18, s[4:5]\n"
    "s_lshr_b32 s3, s61, 3\n"
    "s_lshl_b32 s62, s60, 2\n"
    "s_add_u32 s62, s62, s3\n"
    "s_and_b32 s63, s61, 7\n"
    "s_lshr_b32 s9, s61, 2\n"
    "s_lshr_b32 s64, s60, 1\n"
    "s_lshl_b32 s64, s64, 3\n"
    "s_add_u32 s64, s64, s9\n"
    "s_and_b32 s8, s61, 3\n"
    "s_and_b32 s65, s60, 1\n"
    "s_lshl_b32 s65, s65, 2\n"
    "s_add_u32 s65, s65, s8\n"
    "s_and_b32 s7, s9, 1\n"
    "s_mul_i32 s7, s7, 2\n"
    "s_add_u32 s66, s7, s65\n"
    "s_and_b32 s66, s66, 3\n"
    "s_lshl_b32 s66, s66, 2\n"
    "v_mov_b32 v13, 0\n"
    "v_lshrrev_b32 v14, 4, v0\n"
    "v_and_b32 v20, 15, v0\n"
    "v_lshrrev_b32 v17, 1, v16\n"
    "v_mul_u32_u24 v28, 0x1800, v17\n"
    "v_and_b32 v22, 1, v16\n"
    "v_lshl_add_u32 v28, v22, 4, v28\n"
    "v_lshlrev_b32 v29, 1, v17\n"
    "s_lshl_b32 s94, s60, 3\n"
    "s_add_u32 s94, s94, s63\n"
    "s_mul_i32 s95, s3, 0xc0000\n"
    "s_lshl_b32 s8, s94, 5\n"
    "s_add_u32 s7, s95, s8\n"
    "s_lshl_b32 s8, s3, 8\n"
    "s_lshl_b32 s9, s6, 2\n"
    "v_add_u32 v21, s9, v14\n"
    "v_lshlrev_b32 v22, 10, v21\n"
    "s_lshl_b32 s9, s66, 2\n"
    "v_add3_u32 v17, v20, s9, 0\n"
    "v_and_b32 v17, 63, v17\n"
    "v_lshl_add_u32 v24, v17, 4, v22\n"
    "v_add3_u32 v17, v20, s9, 16\n"
    "v_and_b32 v17, 63, v17\n"
    "v_lshl_add_u32 v25, v17, 4, v22\n"
    "v_add3_u32 v17, v20, s9, 32\n"
    "v_and_b32 v17, 63, v17\n"
    "v_lshl_add_u32 v26, v17, 4, v22\n"
    "v_add3_u32 v17, v20, s9, 48\n"
    "v_and_b32 v17, 63, v17\n"
    "v_lshl_add_u32 v27, v17, 4, v22\n"
    "s_lshl_b32 s9, s64, 16\n"
    "s_add_i32 s84, s9, 0xffffc000\n"
    "s_add_i32 s85, s9, 0\n"
    "s_add_i32 s86, s9, 0x4000\n"
    "s_add_i32 s87, s9, 0x8000\n"
    "s_add_i32 s88, s9, 0xc000\n"
    "s_max_i32 s84, s84, 0\n"
    "s_waitcnt lgkmcnt(0)\n"
    "buffer_load_ushort v32, v29, s[56:59], s8 offen\n"
    "buffer_load_dwordx4 v[40:43], v28, s[36:39], s7 offen\n"
    "buffer_load_dword v30, v13, s[40:43], 0 offen sc1\n"
    "s_lshl_b32 s11, s2, 2\n"
    "s_add_u32 s11, s11, s6\n"
    "s_mul_i32 s11, s11, 12\n"
    "v_min_u32 v37, 11, v0\n"
    "v_add_u32 v37, s11, v37\n"
    "v_lshlrev_b32 v37, 7, v37\n"
    "buffer_load_dword v18, v37, s[20:23], 0 offen\n"
    "s_lshl_b32 s11, s2, 2\n"
    "s_add_u32 s11, s11, s6\n"
    "s_mul_i32 s11, s11, 4\n"
    "v_min_u32 v37, 3, v0\n"
    "v_add_u32 v37, s11, v37\n"
    "v_lshlrev_b32 v37, 7, v37\n"
    "buffer_load_dword v39, v37, s[24:27], 0 offen\n"
    "buffer_load_dwordx4 v[100:103], v24, s[16:19], s84 offen\n"
    "buffer_load_dwordx4 v[108:111], v24, s[16:19], s85 offen\n"
    "buffer_load_dwordx4 v[116:119], v24, s[16:19], s86 offen\n"
    "s_mov_b32 s92, 0\n"
    "s_add_u32 s68, s66, 0\n"
    "s_and_b32 s68, s68, 15\n"
    "s_add_u32 s69, s66, 1\n"
    "s_and_b32 s69, s69, 15\n"
    "s_add_u32 s70, s66, 2\n"
    "s_and_b32 s70, s70, 15\n"
    "s_add_u32 s71, s66, 3\n"
    "s_and_b32 s71, s71, 15\n"
    "s_add_u32 s72, s66, 4\n"
    "s_and_b32 s72, s72, 15\n"
    "s_add_u32 s73, s66, 5\n"
    "s_and_b32 s73, s73, 15\n"
    "s_add_u32 s74, s66, 6\n"
    "s_and_b32 s74, s74, 15\n"
    "s_add_u32 s75, s66, 7\n"
    "s_and_b32 s75, s75, 15\n"
    "s_add_u32 s76, s66, 8\n"
    "s_and_b32 s76, s76, 15\n"
    "s_add_u32 s77, s66, 9\n"
    "s_and_b32 s77, s77, 15\n"
    "s_add_u32 s78, s66, 10\n"
    "s_and_b32 s78, s78, 15\n"
    "s_add_u32 s79, s66, 11\n"
    "s_and_b32 s79, s79, 15\n"
    "s_add_u32 s80, s66, 12\n"
    "s_and_b32 s80, s80, 15\n"
    "s_add_u32 s81, s66, 13\n"
    "s_and_b32 s81, s81, 15\n"
    "s_add_u32 s82, s66, 14\n"
    "s_and_b32 s82, s82, 15\n"
    "s_add_u32 s83, s66, 15\n"
    "s_and_b32 s83, s83, 15\n"
    "s_mov_b32 s89, 0x1bc0\n"
    "s_mov_b32 s90, 0x3780\n"
    "s_mov_b32 s91, 0x5340\n"
    "v_lshlrev_b32 v1, 4, v0\n"
    "v_and_b32 v17, 7, v20\n"
    "v_xor_b32 v17, v17, v21\n"
    "v_lshlrev_b32 v17, 4, v17\n"
    "v_lshl_add_u32 v2, v20, 8, v17\n"
    "v_add_u32 v3, 0xf000, v2\n"
    "v_lshrrev_b32 v21, 3, v0\n"
    "s_lshl_b32 s7, s6, 3\n"
    "v_add_u32 v22, s7, v21\n"
    "v_mul_u32_u24 v11, 0xc00, v22\n"
    "v_bfe_u32 v21, v21, 1, 1\n"
    "s_and_b32 s7, s6, 1\n"
    "s_lshl_b32 s7, s7, 1\n"
    "v_or_b32 v21, s7, v21\n"
    "v_bfe_u32 v22, v0, 1, 2\n"
    "v_xor_b32 v21, v21, v22\n"
    "v_lshl_add_u32 v11, v21, 5, v11\n"
    "v_and_b32 v21, 1, v0\n"
    "v_lshl_add_u32 v11, v21, 4, v11\n"
    "s_lshl_b32 s7, s65, 7\n"
    "v_add_u32 v11, s7, v11\n"
    "s_mul_i32 s93, s6, 0xc00\n"
    "v_add_u32 v21, 0, v14\n"
    "v_xor_b32 v21, v21, v20\n"
    "v_lshlrev_b32 v21, 4, v21\n"
    "v_lshl_add_u32 v4, v14, 8, v21\n"
    "v_add_u32 v6, 0xf000, v4\n"
    "v_add_u32 v21, 4, v14\n"
    "v_xor_b32 v21, v21, v20\n"
    "v_lshlrev_b32 v21, 4, v21\n"
    "v_lshl_add_u32 v5, v14, 8, v21\n"
    "v_add_u32 v7, 0xf000, v5\n"
    "v_bfe_u32 v21, v0, 2, 2\n"
    "v_mul_u32_u24 v8, 0xc00, v14\n"
    "v_lshl_add_u32 v8, v21, 7, v8\n"
    "v_lshrrev_b32 v21, 1, v21\n"
    "v_and_b32 v22, 1, v14\n"
    "v_lshl_or_b32 v21, v22, 1, v21\n"
    "v_xor_b32 v21, s6, v21\n"
    "v_lshl_add_u32 v8, v21, 5, v8\n"
    "v_and_b32 v21, 3, v0\n"
    "v_lshl_add_u32 v8, v21, 3, v8\n"
    "v_add_u32 v9, 0x10000, v8\n"
    "v_add_u32 v10, 0x20000, v8\n"
    "v_lshlrev_b32 v12, 4, v20\n"
    "v_add_u32 v12, 0x26000, v12\n"
    "s_lshl_b32 s9, s6, 11\n"
    "v_add_u32 v31, s9, v1\n"
    "s_lshl_b32 s9, s66, 5\n"
    "s_lshl_b32 s7, s6, 8\n"
    "s_sub_u32 s7, s7, s9\n"
    "s_lshr_b32 s9, s6, 1\n"
    "s_lshl_b32 s9, s9, 11\n"
    "s_add_u32 s9, s9, 0x26000\n"
    "v_lshl_add_u32 v22, v0, 1, s7\n"
    "v_and_b32 v22, 0x1ff, v22\n"
    "v_and_b32 v21, 0x183, v22\n"
    "v_lshl_add_u32 v23, v21, 2, s9\n"
    "v_bfe_u32 v21, v22, 3, 4\n"
    "v_lshl_add_u32 v23, v21, 4, v23\n"
    "v_bfe_u32 v21, v22, 2, 1\n"
    "v_lshl_add_u32 v23, v21, 8, v23\n"
    "v_lshl_add_u32 v22, v0, 1, s7\n"
    "v_add_u32 v22, 0x80, v22\n"
    "v_and_b32 v22, 0x1ff, v22\n"
    "v_and_b32 v21, 0x183, v22\n"
    "v_lshl_add_u32 v38, v21, 2, s9\n"
    "v_bfe_u32 v21, v22, 3, 4\n"
    "v_lshl_add_u32 v38, v21, 4, v38\n"
    "v_bfe_u32 v21, v22, 2, 1\n"
    "v_lshl_add_u32 v38, v21, 8, v38\n"
    "s_mul_i32 s96, s3, 0x6000\n"
    "s_add_u32 s96, s96, 0x60000\n"
    "s_lshl_b32 s10, s94, 7\n"
    "s_add_u32 s10, s10, s96\n"
    "s_lshl_b32 s100, s3, 8\n"
    "s_mov_b32 s62, s64\n"
    "s_mov_b32 s63, s65\n"
    "s_lshr_b32 s3, s61, 2\n"
    "s_mov_b32 s97, 0\n"
    "s_mul_i32 s9, s6, 0x100\n"
    "s_add_u32 s9, s9, 0x27000\n"
    "v_and_b32 v59, 1, v0\n"
    "v_lshlrev_b32 v59, 5, v59\n"
    "v_lshl_add_u32 v59, v14, 6, v59\n"
    "v_add_u32 v59, s9, v59\n"
    "v_and_b32 v57, 15, v0\n"
    "v_lshrrev_b32 v58, 4, v0\n"
    "v_mul_u32_u24 v58, 0x100, v58\n"
    "v_lshl_add_u32 v58, v57, 2, v58\n"
    "v_add_u32 v58, 0x27000, v58\n"
    "v_lshlrev_b32 v57, 3, v57\n"
    "buffer_load_dwordx4 v[124:127], v24, s[16:19], s87 offen\n"
    "buffer_load_dwordx4 v[132:135], v24, s[16:19], s88 offen\n"
    "v_lshrrev_b32 v17, 2, v16\n"
    "v_lshlrev_b32 v17, 1, v17\n"
    "buffer_load_ushort a254, v17, s[56:59], s100 offen\n"
    "buffer_load_ushort a255, v17, s[56:59], s100 offen offset:128\n"
    "s_waitcnt vmcnt(11)\n"
    "v_lshlrev_b32 v32, 16, v32\n"
    "v_mul_f32 v33, 0xbfb8aa3b, v32\n"
    "v_exp_f32 v33, v33\n"
    "s_nop 0\n"
    "v_add_f32 v33, 1.0, v33\n"
    "v_rcp_f32 v33, v33\n"
    "s_nop 0\n"
    "v_mul_f32 v32, v32, v33\n"
    "v_cvt_pk_bf16_f32 v32, v32, v32\n"
    "v_and_b32 v32, 0xffff0000, v32\n"
    "s_waitcnt vmcnt(10)\n"
    "v_lshlrev_b32 v44, 16, v40\n"
    "v_and_b32 v45, 0xffff0000, v40\n"
    "v_lshlrev_b32 v46, 16, v41\n"
    "v_and_b32 v47, 0xffff0000, v41\n"
    "v_lshlrev_b32 v48, 16, v42\n"
    "v_and_b32 v49, 0xffff0000, v42\n"
    "v_lshlrev_b32 v50, 16, v43\n"
    "v_and_b32 v51, 0xffff0000, v43\n"
    "v_pk_mul_f32 v[44:45], v[44:45], v[32:33] op_sel_hi:[1,0]\n"
    "v_pk_mul_f32 v[46:47], v[46:47], v[32:33] op_sel_hi:[1,0]\n"
    "v_pk_mul_f32 v[48:49], v[48:49], v[32:33] op_sel_hi:[1,0]\n"
    "v_pk_mul_f32 v[50:51], v[50:51], v[32:33] op_sel_hi:[1,0]\n"
    "s_waitcnt vmcnt(9)\n"
    "v_readfirstlane_b32 s67, v30\n"
    "s_nop 0\n"
    "s_add_u32 s67, s67, 0x9e3779b9\n"
    "v_add_f32_dpp v44, v44, v44 row_ror:2 row_mask:0xf bank_mask:0xf\n"
    "v_add_f32_dpp v45, v45, v45 row_ror:2 row_mask:0xf bank_mask:0xf\n"
    "v_add_f32_dpp v46, v46, v46 row_ror:2 row_mask:0xf bank_mask:0xf\n"
    "v_add_f32_dpp v47, v47, v47 row_ror:2 row_mask:0xf bank_mask:0xf\n"
    "v_add_f32_dpp v48, v48, v48 row_ror:2 row_mask:0xf bank_mask:0xf\n"
    "v_add_f32_dpp v49, v49, v49 row_ror:2 row_mask:0xf bank_mask:0xf\n"
    "v_add_f32_dpp v50, v50, v50 row_ror:2 row_mask:0xf bank_mask:0xf\n"
    "v_add_f32_dpp v51, v51, v51 row_ror:2 row_mask:0xf bank_mask:0xf\n"
    "v_add_f32_dpp v44, v44, v44 row_ror:4 row_mask:0xf bank_mask:0xf\n"
    "v_add_f32_dpp v45, v45, v45 row_ror:4 row_mask:0xf bank_mask:0xf\n"
    "v_add_f32_dpp v46, v46, v46 row_ror:4 row_mask:0xf bank_mask:0xf\n"
    "v_add_f32_dpp v47, v47, v47 row_ror:4 row_mask:0xf bank_mask:0xf\n"
    "v_add_f32_dpp v48, v48, v48 row_ror:4 row_mask:0xf bank_mask:0xf\n"
    "v_add_f32_dpp v49, v49, v49 row_ror:4 row_mask:0xf bank_mask:0xf\n"
    "v_add_f32_dpp v50, v50, v50 row_ror:4 row_mask:0xf bank_mask:0xf\n"
    "v_add_f32_dpp v51, v51, v51 row_ror:4 row_mask:0xf bank_mask:0xf\n"
    "v_add_f32_dpp v44, v44, v44 row_ror:8 row_mask:0xf bank_mask:0xf\n"
    "v_add_f32_dpp v45, v45, v45 row_ror:8 row_mask:0xf bank_mask:0xf\n"
    "v_add_f32_dpp v46, v46, v46 row_ror:8 row_mask:0xf bank_mask:0xf\n"
    "v_add_f32_dpp v47, v47, v47 row_ror:8 row_mask:0xf bank_mask:0xf\n"
    "v_add_f32_dpp v48, v48, v48 row_ror:8 row_mask:0xf bank_mask:0xf\n"
    "v_add_f32_dpp v49, v49, v49 row_ror:8 row_mask:0xf bank_mask:0xf\n"
    "v_add_f32_dpp v50, v50, v50 row_ror:8 row_mask:0xf bank_mask:0xf\n"
    "v_add_f32_dpp v51, v51, v51 row_ror:8 row_mask:0xf bank_mask:0xf\n"
    "s_mov_b32 exec_lo, 0x30003\n"
    "s_mov_b32 exec_hi, 0x30003\n"
    "ds_write_b128 v59, v[44:47]\n"
    "ds_write_b128 v59, v[48:51] offset:16\n"
    "s_mov_b64 exec, -1\n"
    "s_waitcnt lgkmcnt(0)\n"
    "s_barrier\n"
    "s_cmp_eq_u32 s6, 0\n"
    "s_cbranch_scc0 L_mod1\n"
    "ds_read_b32 v80, v58\n"
    "ds_read_b32 v81, v58 offset:64\n"
    "ds_read_b32 v82, v58 offset:128\n"
    "ds_read_b32 v83, v58 offset:192\n"
    "s_waitcnt lgkmcnt(0)\n"
    "v_pk_add_f32 v[80:81], v[80:81], v[82:83]\n"
    "v_add_f32 v80, v80, v81\n"
    "v_mov_b32 v81, v80\n"
    "s_nop 1\n"
    "v_permlane32_swap_b32 v80, v81\n"
    "v_add_f32 v80, v80, v81\n"
    "v_mov_b32 v81, v80\n"
    "s_nop 1\n"
    "v_permlane16_swap_b32 v80, v81\n"
    "v_add_f32 v80, v80, v81\n"
    "v_mov_b32 v81, s67\n"
    "s_mov_b64 exec, 0xffff\n"
    "buffer_store_dwordx2 v[80:81], v57, s[40:43], s10 offen sc1\n"
    "s_mov_b64 exec, -1\n"
    "L_mod1:\n"
    "buffer_load_dwordx4 v[140:143], v25, s[16:19], s84 offen\n"
    "buffer_load_dwordx4 v[148:151], v25, s[16:19], s85 offen\n"
    "buffer_load_dwordx4 v[156:159], v25, s[16:19], s86 offen\n"
    "buffer_load_dwordx4 v[164:167], v25, s[16:19], s87 offen\n"
    "buffer_load_dwordx4 v[172:175], v25, s[16:19], s88 offen\n"
    "s_waitcnt vmcnt(11)\n"
    "v_lshlrev_b32 v96, 16, v100\n"
    "v_and_b32 v97, 0xffff0000, v100\n"
    "v_lshlrev_b32 v98, 16, v101\n"
    "v_and_b32 v99, 0xffff0000, v101\n"
    "v_lshlrev_b32 v100, 16, v102\n"
    "v_and_b32 v101, 0xffff0000, v102\n"
    "v_lshlrev_b32 v102, 16, v103\n"
    "v_and_b32 v103, 0xffff0000, v103\n"
    "v_pk_mul_f32 v[70:71], v[96:97], v[96:97]\n"
    "v_pk_fma_f32 v[70:71], v[98:99], v[98:99], v[70:71]\n"
    "v_pk_add_f32 v[60:61], v[96:97], v[98:99]\n"
    "v_pk_fma_f32 v[70:71], v[100:101], v[100:101], v[70:71]\n"
    "v_pk_add_f32 v[60:61], v[60:61], v[100:101]\n"
    "v_pk_fma_f32 v[70:71], v[102:103], v[102:103], v[70:71]\n"
    "v_pk_add_f32 v[60:61], v[60:61], v[102:103]\n"
    "s_waitcnt vmcnt(10)\n"
    "v_lshlrev_b32 v104, 16, v108\n"
    "v_and_b32 v105, 0xffff0000, v108\n"
    "v_lshlrev_b32 v106, 16, v109\n"
    "v_and_b32 v107, 0xffff0000, v109\n"
    "v_lshlrev_b32 v108, 16, v110\n"
    "v_and_b32 v109, 0xffff0000, v110\n"
    "v_lshlrev_b32 v110, 16, v111\n"
    "v_and_b32 v111, 0xffff0000, v111\n"
    "v_pk_mul_f32 v[72:73], v[104:105], v[104:105]\n"
    "v_pk_fma_f32 v[72:73], v[106:107], v[106:107], v[72:73]\n"
    "v_pk_add_f32 v[62:63], v[104:105], v[106:107]\n"
    "v_pk_fma_f32 v[72:73], v[108:109], v[108:109], v[72:73]\n"
    "v_pk_add_f32 v[62:63], v[62:63], v[108:109]\n"
    "v_pk_fma_f32 v[72:73], v[110:111], v[110:111], v[72:73]\n"
    "v_pk_add_f32 v[62:63], v[62:63], v[110:111]\n"
    "buffer_load_dwordx4 v[180:183], v26, s[16:19], s84 offen\n"
    "buffer_load_dwordx4 v[188:191], v26, s[16:19], s85 offen\n"
    "buffer_load_dwordx4 v[196:199], v26, s[16:19], s86 offen\n"
    "buffer_load_dwordx4 v[204:207], v26, s[16:19], s87 offen\n"
    "buffer_load_dwordx4 v[212:215], v26, s[16:19], s88 offen\n"
    "s_waitcnt vmcnt(14)\n"
    "v_lshlrev_b32 v112, 16, v116\n"
    "v_and_b32 v113, 0xffff0000, v116\n"
    "v_lshlrev_b32 v114, 16, v117\n"
    "v_and_b32 v115, 0xffff0000, v117\n"
    "v_lshlrev_b32 v116, 16, v118\n"
    "v_and_b32 v117, 0xffff0000, v118\n"
    "v_lshlrev_b32 v118, 16, v119\n"
    "v_and_b32 v119, 0xffff0000, v119\n"
    "v_pk_mul_f32 v[74:75], v[112:113], v[112:113]\n"
    "v_pk_fma_f32 v[74:75], v[114:115], v[114:115], v[74:75]\n"
    "v_pk_add_f32 v[64:65], v[112:113], v[114:115]\n"
    "v_pk_fma_f32 v[74:75], v[116:117], v[116:117], v[74:75]\n"
    "v_pk_add_f32 v[64:65], v[64:65], v[116:117]\n"
    "v_pk_fma_f32 v[74:75], v[118:119], v[118:119], v[74:75]\n"
    "v_pk_add_f32 v[64:65], v[64:65], v[118:119]\n"
    "s_waitcnt vmcnt(13)\n"
    "v_lshlrev_b32 v120, 16, v124\n"
    "v_and_b32 v121, 0xffff0000, v124\n"
    "v_lshlrev_b32 v122, 16, v125\n"
    "v_and_b32 v123, 0xffff0000, v125\n"
    "v_lshlrev_b32 v124, 16, v126\n"
    "v_and_b32 v125, 0xffff0000, v126\n"
    "v_lshlrev_b32 v126, 16, v127\n"
    "v_and_b32 v127, 0xffff0000, v127\n"
    "v_pk_mul_f32 v[76:77], v[120:121], v[120:121]\n"
    "v_pk_fma_f32 v[76:77], v[122:123], v[122:123], v[76:77]\n"
    "v_pk_add_f32 v[66:67], v[120:121], v[122:123]\n"
    "v_pk_fma_f32 v[76:77], v[124:125], v[124:125], v[76:77]\n"
    "v_pk_add_f32 v[66:67], v[66:67], v[124:125]\n"
    "v_pk_fma_f32 v[76:77], v[126:127], v[126:127], v[76:77]\n"
    "v_pk_add_f32 v[66:67], v[66:67], v[126:127]\n"
    "s_waitcnt vmcnt(12)\n"
    "v_lshlrev_b32 v128, 16, v132\n"
    "v_and_b32 v129, 0xffff0000, v132\n"
    "v_lshlrev_b32 v130, 16, v133\n"
    "v_and_b32 v131, 0xffff0000, v133\n"
    "v_lshlrev_b32 v132, 16, v134\n"
    "v_and_b32 v133, 0xffff0000, v134\n"
    "v_lshlrev_b32 v134, 16, v135\n"
    "v_and_b32 v135, 0xffff0000, v135\n"
    "v_pk_mul_f32 v[78:79], v[128:129], v[128:129]\n"
    "v_pk_fma_f32 v[78:79], v[130:131], v[130:131], v[78:79]\n"
    "buffer_load_dwordx4 v[220:223], v27, s[16:19], s84 offen\n"
    "buffer_load_dwordx4 v[228:231], v27, s[16:19], s85 offen\n"
    "buffer_load_dwordx4 v[236:239], v27, s[16:19], s86 offen\n"
    "buffer_load_dwordx4 v[244:247], v27, s[16:19], s87 offen\n"
    "buffer_load_dwordx4 v[252:255], v27, s[16:19], s88 offen\n"
    "s_and_b32 s86, s97, 3\n"
    "s_mov_b32 s61, 0\n"
    "s_add_u32 s87, s6, s61\n"
    "s_and_b32 s87, s87, 3\n"
    "v_pk_add_f32 v[68:69], v[128:129], v[130:131]\n"
    "v_pk_fma_f32 v[78:79], v[132:133], v[132:133], v[78:79]\n"
    "v_pk_add_f32 v[68:69], v[68:69], v[132:133]\n"
    "v_pk_fma_f32 v[78:79], v[134:135], v[134:135], v[78:79]\n"
    "v_pk_add_f32 v[68:69], v[68:69], v[134:135]\n"
    "s_waitcnt vmcnt(14)\n"
    "v_lshlrev_b32 v136, 16, v140\n"
    "v_and_b32 v137, 0xffff0000, v140\n"
    "v_lshlrev_b32 v138, 16, v141\n"
    "v_and_b32 v139, 0xffff0000, v141\n"
    "v_lshlrev_b32 v140, 16, v142\n"
    "v_and_b32 v141, 0xffff0000, v142\n"
    "v_lshlrev_b32 v142, 16, v143\n"
    "v_and_b32 v143, 0xffff0000, v143\n"
    "v_pk_fma_f32 v[70:71], v[136:137], v[136:137], v[70:71]\n"
    "v_pk_add_f32 v[60:61], v[60:61], v[136:137]\n"
    "v_pk_fma_f32 v[70:71], v[138:139], v[138:139], v[70:71]\n"
    "v_pk_add_f32 v[60:61], v[60:61], v[138:139]\n"
    "v_pk_fma_f32 v[70:71], v[140:141], v[140:141], v[70:71]\n"
    "v_pk_add_f32 v[60:61], v[60:61], v[140:141]\n"
    "v_pk_fma_f32 v[70:71], v[142:143], v[142:143], v[70:71]\n"
    "s_add_u32 m0, s93, 0x14000\n"
    "s_mul_i32 s10, s68, 0x18000\n"
    "buffer_load_dwordx4 v11, s[20:23], s10 offen lds\n"
    "v_pk_add_f32 v[60:61], v[60:61], v[142:143]\n"
    "s_waitcnt vmcnt(14)\n"
    "v_lshlrev_b32 v144, 16, v148\n"
    "v_and_b32 v145, 0xffff0000, v148\n"
    "v_lshlrev_b32 v146, 16, v149\n"
    "v_and_b32 v147, 0xffff0000, v149\n"
    "v_lshlrev_b32 v148, 16, v150\n"
    "v_and_b32 v149, 0xffff0000, v150\n"
    "v_lshlrev_b32 v150, 16, v151\n"
    "v_and_b32 v151, 0xffff0000, v151\n"
    "v_pk_fma_f32 v[72:73], v[144:145], v[144:145], v[72:73]\n"
    "v_pk_add_f32 v[62:63], v[62:63], v[144:145]\n"
    "v_pk_fma_f32 v[72:73], v[146:147], v[146:147], v[72:73]\n"
    "v_pk_add_f32 v[62:63], v[62:63], v[146:147]\n"
    "v_pk_fma_f32 v[72:73], v[148:149], v[148:149], v[72:73]\n"
    "v_pk_add_f32 v[62:63], v[62:63], v[148:149]\n"
    "v_pk_fma_f32 v[72:73], v[150:151], v[150:151], v[72:73]\n"
    "v_pk_add_f32 v[62:63], v[62:63], v[150:151]\n"
    "s_waitcnt vmcnt(13)\n"
    "v_lshlrev_b32 v152, 16, v156\n"
    "v_and_b32 v153, 0xffff0000, v156\n"
    "v_lshlrev_b32 v154, 16, v157\n"
    "buffer_load_dwordx4 v11, s[20:23], s10 offen offset:1024 lds\n"
    "v_and_b32 v155, 0xffff0000, v157\n"
    "v_lshlrev_b32 v156, 16, v158\n"
    "v_and_b32 v157, 0xffff0000, v158\n"
    "v_lshlrev_b32 v158, 16, v159\n"
    "v_and_b32 v159, 0xffff0000, v159\n"
    "v_pk_fma_f32 v[74:75], v[152:153], v[152:153], v[74:75]\n"
    "v_pk_add_f32 v[64:65], v[64:65], v[152:153]\n"
    "v_pk_fma_f32 v[74:75], v[154:155], v[154:155], v[74:75]\n"
    "v_pk_add_f32 v[64:65], v[64:65], v[154:155]\n"
    "v_pk_fma_f32 v[74:75], v[156:157], v[156:157], v[74:75]\n"
    "v_pk_add_f32 v[64:65], v[64:65], v[156:157]\n"
    "v_pk_fma_f32 v[74:75], v[158:159], v[158:159], v[74:75]\n"
    "v_pk_add_f32 v[64:65], v[64:65], v[158:159]\n"
    "s_waitcnt vmcnt(13)\n"
    "v_lshlrev_b32 v160, 16, v164\n"
    "v_and_b32 v161, 0xffff0000, v164\n"
    "v_lshlrev_b32 v162, 16, v165\n"
    "v_and_b32 v163, 0xffff0000, v165\n"
    "v_lshlrev_b32 v164, 16, v166\n"
    "v_and_b32 v165, 0xffff0000, v166\n"
    "v_lshlrev_b32 v166, 16, v167\n"
    "buffer_load_dwordx4 v11, s[20:23], s10 offen offset:2048 lds\n"
    "v_and_b32 v167, 0xffff0000, v167\n"
    "v_pk_fma_f32 v[76:77], v[160:161], v[160:161], v[76:77]\n"
    "v_pk_add_f32 v[66:67], v[66:67], v[160:161]\n"
    "v_pk_fma_f32 v[76:77], v[162:163], v[162:163], v[76:77]\n"
    "v_pk_add_f32 v[66:67], v[66:67], v[162:163]\n"
    "v_pk_fma_f32 v[76:77], v[164:165], v[164:165], v[76:77]\n"
    "v_pk_add_f32 v[66:67], v[66:67], v[164:165]\n"
    "v_pk_fma_f32 v[76:77], v[166:167], v[166:167], v[76:77]\n"
    "v_pk_add_f32 v[66:67], v[66:67], v[166:167]\n"
    "s_waitcnt vmcnt(13)\n"
    "v_lshlrev_b32 v168, 16, v172\n"
    "v_and_b32 v169, 0xffff0000, v172\n"
    "v_lshlrev_b32 v170, 16, v173\n"
    "v_and_b32 v171, 0xffff0000, v173\n"
    "v_lshlrev_b32 v172, 16, v174\n"
    "v_and_b32 v173, 0xffff0000, v174\n"
    "v_lshlrev_b32 v174, 16, v175\n"
    "v_and_b32 v175, 0xffff0000, v175\n"
    "v_pk_fma_f32 v[78:79], v[168:169], v[168:169], v[78:79]\n"
    "v_pk_add_f32 v[68:69], v[68:69], v[168:169]\n"
    "v_pk_fma_f32 v[78:79], v[170:171], v[170:171], v[78:79]\n"
    "s_add_u32 m0, s93, 0x17000\n"
    "s_mul_i32 s10, s69, 0x18000\n"
    "buffer_load_dwordx4 v11, s[20:23], s10 offen lds\n"
    "v_pk_add_f32 v[68:69], v[68:69], v[170:171]\n"
    "v_pk_fma_f32 v[78:79], v[172:173], v[172:173], v[78:79]\n"
    "v_pk_add_f32 v[68:69], v[68:69], v[172:173]\n"
    "v_pk_fma_f32 v[78:79], v[174:175], v[174:175], v[78:79]\n"
    "v_pk_add_f32 v[68:69], v[68:69], v[174:175]\n"
    "s_waitcnt vmcnt(13)\n"
    "v_lshlrev_b32 v176, 16, v180\n"
    "v_and_b32 v177, 0xffff0000, v180\n"
    "v_lshlrev_b32 v178, 16, v181\n"
    "v_and_b32 v179, 0xffff0000, v181\n"
    "v_lshlrev_b32 v180, 16, v182\n"
    "v_and_b32 v181, 0xffff0000, v182\n"
    "v_lshlrev_b32 v182, 16, v183\n"
    "v_and_b32 v183, 0xffff0000, v183\n"
    "v_pk_fma_f32 v[70:71], v[176:177], v[176:177], v[70:71]\n"
    "v_pk_add_f32 v[60:61], v[60:61], v[176:177]\n"
    "v_pk_fma_f32 v[70:71], v[178:179], v[178:179], v[70:71]\n"
    "v_pk_add_f32 v[60:61], v[60:61], v[178:179]\n"
    "v_pk_fma_f32 v[70:71], v[180:181], v[180:181], v[70:71]\n"
    "v_pk_add_f32 v[60:61], v[60:61], v[180:181]\n"
    "v_pk_fma_f32 v[70:71], v[182:183], v[182:183], v[70:71]\n"
    "buffer_load_dwordx4 v11, s[20:23], s10 offen offset:1024 lds\n"
    "v_pk_add_f32 v[60:61], v[60:61], v[182:183]\n"
    "s_waitcnt vmcnt(13)\n"
    "v_lshlrev_b32 v184, 16, v188\n"
    "v_and_b32 v185, 0xffff0000, v188\n"
    "v_lshlrev_b32 v186, 16, v189\n"
    "v_and_b32 v187, 0xffff0000, v189\n"
    "v_lshlrev_b32 v188, 16, v190\n"
    "v_and_b32 v189, 0xffff0000, v190\n"
    "v_lshlrev_b32 v190, 16, v191\n"
    "v_and_b32 v191, 0xffff0000, v191\n"
    "v_pk_fma_f32 v[72:73], v[184:185], v[184:185], v[72:73]\n"
    "v_pk_add_f32 v[62:63], v[62:63], v[184:185]\n"
    "v_pk_fma_f32 v[72:73], v[186:187], v[186:187], v[72:73]\n"
    "v_pk_add_f32 v[62:63], v[62:63], v[186:187]\n"
    "v_pk_fma_f32 v[72:73], v[188:189], v[188:189], v[72:73]\n"
    "v_pk_add_f32 v[62:63], v[62:63], v[188:189]\n"
    "v_pk_fma_f32 v[72:73], v[190:191], v[190:191], v[72:73]\n"
    "v_pk_add_f32 v[62:63], v[62:63], v[190:191]\n"
    "s_waitcnt vmcnt(12)\n"
    "v_lshlrev_b32 v192, 16, v196\n"
    "v_and_b32 v193, 0xffff0000, v196\n"
    "v_lshlrev_b32 v194, 16, v197\n"
    "buffer_load_dwordx4 v11, s[20:23], s10 offen offset:2048 lds\n"
    "v_and_b32 v195, 0xffff0000, v197\n"
    "v_lshlrev_b32 v196, 16, v198\n"
    "v_and_b32 v197, 0xffff0000, v198\n"
    "v_lshlrev_b32 v198, 16, v199\n"
    "v_and_b32 v199, 0xffff0000, v199\n"
    "v_pk_fma_f32 v[74:75], v[192:193], v[192:193], v[74:75]\n"
    "v_pk_add_f32 v[64:65], v[64:65], v[192:193]\n"
    "v_pk_fma_f32 v[74:75], v[194:195], v[194:195], v[74:75]\n"
    "v_pk_add_f32 v[64:65], v[64:65], v[194:195]\n"
    "v_pk_fma_f32 v[74:75], v[196:197], v[196:197], v[74:75]\n"
    "v_pk_add_f32 v[64:65], v[64:65], v[196:197]\n"
    "v_pk_fma_f32 v[74:75], v[198:199], v[198:199], v[74:75]\n"
    "v_pk_add_f32 v[64:65], v[64:65], v[198:199]\n"
    "s_waitcnt vmcnt(12)\n"
    "v_lshlrev_b32 v200, 16, v204\n"
    "v_and_b32 v201, 0xffff0000, v204\n"
    "v_lshlrev_b32 v202, 16, v205\n"
    "v_and_b32 v203, 0xffff0000, v205\n"
    "v_lshlrev_b32 v204, 16, v206\n"
    "v_and_b32 v205, 0xffff0000, v206\n"
    "v_lshlrev_b32 v206, 16, v207\n"
    "s_add_u32 m0, s93, 0x1a000\n"
    "s_mul_i32 s10, s70, 0x18000\n"
    "buffer_load_dwordx4 v11, s[20:23], s10 offen lds\n"
    "v_and_b32 v207, 0xffff0000, v207\n"
    "v_pk_fma_f32 v[76:77], v[200:201], v[200:201], v[76:77]\n"
    "v_pk_add_f32 v[66:67], v[66:67], v[200:201]\n"
    "v_pk_fma_f32 v[76:77], v[202:203], v[202:203], v[76:77]\n"
    "v_pk_add_f32 v[66:67], v[66:67], v[202:203]\n"
    "v_pk_fma_f32 v[76:77], v[204:205], v[204:205], v[76:77]\n"
    "v_pk_add_f32 v[66:67], v[66:67], v[204:205]\n"
    "v_pk_fma_f32 v[76:77], v[206:207], v[206:207], v[76:77]\n"
    "v_pk_add_f32 v[66:67], v[66:67], v[206:207]\n"
    "s_waitcnt vmcnt(12)\n"
    "v_lshlrev_b32 v208, 16, v212\n"
    "v_and_b32 v209, 0xffff0000, v212\n"
    "v_lshlrev_b32 v210, 16, v213\n"
    "v_and_b32 v211, 0xffff0000, v213\n"
    "v_lshlrev_b32 v212, 16, v214\n"
    "v_and_b32 v213, 0xffff0000, v214\n"
    "v_lshlrev_b32 v214, 16, v215\n"
    "v_and_b32 v215, 0xffff0000, v215\n"
    "v_pk_fma_f32 v[78:79], v[208:209], v[208:209], v[78:79]\n"
    "v_pk_add_f32 v[68:69], v[68:69], v[208:209]\n"
    "v_pk_fma_f32 v[78:79], v[210:211], v[210:211], v[78:79]\n"
    "buffer_load_dwordx4 v11, s[20:23], s10 offen offset:1024 lds\n"
    "v_pk_add_f32 v[68:69], v[68:69], v[210:211]\n"
    "v_pk_fma_f32 v[78:79], v[212:213], v[212:213], v[78:79]\n"
    "v_pk_add_f32 v[68:69], v[68:69], v[212:213]\n"
    "v_pk_fma_f32 v[78:79], v[214:215], v[214:215], v[78:79]\n"
    "v_pk_add_f32 v[68:69], v[68:69], v[214:215]\n"
    "s_waitcnt vmcnt(12)\n"
    "v_lshlrev_b32 v216, 16, v220\n"
    "v_and_b32 v217, 0xffff0000, v220\n"
    "v_lshlrev_b32 v218, 16, v221\n"
    "v_and_b32 v219, 0xffff0000, v221\n"
    "v_lshlrev_b32 v220, 16, v222\n"
    "v_and_b32 v221, 0xffff0000, v222\n"
    "v_lshlrev_b32 v222, 16, v223\n"
    "v_and_b32 v223, 0xffff0000, v223\n"
    "v_pk_fma_f32 v[70:71], v[216:217], v[216:217], v[70:71]\n"
    "v_pk_add_f32 v[60:61], v[60:61], v[216:217]\n"
    "v_pk_fma_f32 v[70:71], v[218:219], v[218:219], v[70:71]\n"
    "v_pk_add_f32 v[60:61], v[60:61], v[218:219]\n"
    "v_pk_fma_f32 v[70:71], v[220:221], v[220:221], v[70:71]\n"
    "v_pk_add_f32 v[60:61], v[60:61], v[220:221]\n"
    "v_pk_fma_f32 v[70:71], v[222:223], v[222:223], v[70:71]\n"
    "buffer_load_dwordx4 v11, s[20:23], s10 offen offset:2048 lds\n"
    "v_pk_add_f32 v[60:61], v[60:61], v[222:223]\n"
    "s_waitcnt vmcnt(12)\n"
    "v_lshlrev_b32 v224, 16, v228\n"
    "v_and_b32 v225, 0xffff0000, v228\n"
    "v_lshlrev_b32 v226, 16, v229\n"
    "v_and_b32 v227, 0xffff0000, v229\n"
    "v_lshlrev_b32 v228, 16, v230\n"
    "v_and_b32 v229, 0xffff0000, v230\n"
    "v_lshlrev_b32 v230, 16, v231\n"
    "v_and_b32 v231, 0xffff0000, v231\n"
    "v_pk_fma_f32 v[72:73], v[224:225], v[224:225], v[72:73]\n"
    "v_pk_add_f32 v[62:63], v[62:63], v[224:225]\n"
    "v_pk_fma_f32 v[72:73], v[226:227], v[226:227], v[72:73]\n"
    "v_pk_add_f32 v[62:63], v[62:63], v[226:227]\n"
    "v_pk_fma_f32 v[72:73], v[228:229], v[228:229], v[72:73]\n"
    "v_pk_add_f32 v[62:63], v[62:63], v[228:229]\n"
    "v_pk_fma_f32 v[72:73], v[230:231], v[230:231], v[72:73]\n"
    "v_pk_add_f32 v[62:63], v[62:63], v[230:231]\n"
    "s_waitcnt vmcnt(11)\n"
    "v_lshlrev_b32 v232, 16, v236\n"
    "v_and_b32 v233, 0xffff0000, v236\n"
    "v_lshlrev_b32 v234, 16, v237\n"
    "s_add_u32 m0, s93, 0x1d000\n"
    "s_mul_i32 s10, s71, 0x18000\n"
    "buffer_load_dwordx4 v11, s[20:23], s10 offen lds\n"
    "v_and_b32 v235, 0xffff0000, v237\n"
    "v_lshlrev_b32 v236, 16, v238\n"
    "v_and_b32 v237, 0xffff0000, v238\n"
    "v_lshlrev_b32 v238, 16, v239\n"
    "v_and_b32 v239, 0xffff0000, v239\n"
    "v_pk_fma_f32 v[74:75], v[232:233], v[232:233], v[74:75]\n"
    "v_pk_add_f32 v[64:65], v[64:65], v[232:233]\n"
    "v_pk_fma_f32 v[74:75], v[234:235], v[234:235], v[74:75]\n"
    "v_pk_add_f32 v[64:65], v[64:65], v[234:235]\n"
    "v_pk_fma_f32 v[74:75], v[236:237], v[236:237], v[74:75]\n"
    "v_pk_add_f32 v[64:65], v[64:65], v[236:237]\n"
    "v_pk_fma_f32 v[74:75], v[238:239], v[238:239], v[74:75]\n"
    "v_pk_add_f32 v[64:65], v[64:65], v[238:239]\n"
    "s_waitcnt vmcnt(11)\n"
    "v_lshlrev_b32 v240, 16, v244\n"
    "v_and_b32 v241, 0xffff0000, v244\n"
    "v_lshlrev_b32 v242, 16, v245\n"
    "v_and_b32 v243, 0xffff0000, v245\n"
    "v_lshlrev_b32 v244, 16, v246\n"
    "v_and_b32 v245, 0xffff0000, v246\n"
    "v_lshlrev_b32 v246, 16, v247\n"
    "buffer_load_dwordx4 v11, s[20:23], s10 offen offset:1024 lds\n"
    "v_and_b32 v247, 0xffff0000, v247\n"
    "v_pk_fma_f32 v[76:77], v[240:241], v[240:241], v[76:77]\n"
    "v_pk_add_f32 v[66:67], v[66:67], v[240:241]\n"
    "v_pk_fma_f32 v[76:77], v[242:243], v[242:243], v[76:77]\n"
    "v_pk_add_f32 v[66:67], v[66:67], v[242:243]\n"
    "v_pk_fma_f32 v[76:77], v[244:245], v[244:245], v[76:77]\n"
    "v_pk_add_f32 v[66:67], v[66:67], v[244:245]\n"
    "v_pk_fma_f32 v[76:77], v[246:247], v[246:247], v[76:77]\n"
    "v_pk_add_f32 v[66:67], v[66:67], v[246:247]\n"
    "s_waitcnt vmcnt(11)\n"
    "v_lshlrev_b32 v248, 16, v252\n"
    "v_and_b32 v249, 0xffff0000, v252\n"
    "v_lshlrev_b32 v250, 16, v253\n"
    "v_and_b32 v251, 0xffff0000, v253\n"
    "v_lshlrev_b32 v252, 16, v254\n"
    "v_and_b32 v253, 0xffff0000, v254\n"
    "v_lshlrev_b32 v254, 16, v255\n"
    "v_and_b32 v255, 0xffff0000, v255\n"
    "v_pk_fma_f32 v[78:79], v[248:249], v[248:249], v[78:79]\n"
    "v_pk_add_f32 v[68:69], v[68:69], v[248:249]\n"
    "v_pk_fma_f32 v[78:79], v[250:251], v[250:251], v[78:79]\n"
    "buffer_load_dwordx4 v11, s[20:23], s10 offen offset:2048 lds\n"
    "v_pk_add_f32 v[68:69], v[68:69], v[250:251]\n"
    "v_pk_fma_f32 v[78:79], v[252:253], v[252:253], v[78:79]\n"
    "v_pk_add_f32 v[68:69], v[68:69], v[252:253]\n"
    "v_pk_fma_f32 v[78:79], v[254:255], v[254:255], v[78:79]\n"
    "v_pk_add_f32 v[68:69], v[68:69], v[254:255]\n"
    "v_add_f32 v60, v60, v61\n"
    "v_add_f32 v70, v70, v71\n"
    "v_add_f32 v62, v62, v63\n"
    "v_add_f32 v72, v72, v73\n"
    "v_add_f32 v64, v64, v65\n"
    "v_add_f32 v74, v74, v75\n"
    "v_add_f32 v66, v66, v67\n"
    "v_add_f32 v76, v76, v77\n"
    "v_add_f32 v68, v68, v69\n"
    "v_add_f32 v78, v78, v79\n"
    "v_add_f32_dpp v60, v60, v60 row_ror:1 row_mask:0xf bank_mask:0xf\n"
    "v_add_f32_dpp v70, v70, v70 row_ror:1 row_mask:0xf bank_mask:0xf\n"
    "v_add_f32_dpp v62, v62, v62 row_ror:1 row_mask:0xf bank_mask:0xf\n"
    "v_add_f32_dpp v72, v72, v72 row_ror:1 row_mask:0xf bank_mask:0xf\n"
    "v_add_f32_dpp v64, v64, v64 row_ror:1 row_mask:0xf bank_mask:0xf\n"
    "v_add_f32_dpp v74, v74, v74 row_ror:1 row_mask:0xf bank_mask:0xf\n"
    "v_add_f32_dpp v66, v66, v66 row_ror:1 row_mask:0xf bank_mask:0xf\n"
    "v_add_f32_dpp v76, v76, v76 row_ror:1 row_mask:0xf bank_mask:0xf\n"
    "v_add_f32_dpp v68, v68, v68 row_ror:1 row_mask:0xf bank_mask:0xf\n"
    "v_add_f32_dpp v78, v78, v78 row_ror:1 row_mask:0xf bank_mask:0xf\n"
    "v_add_f32_dpp v60, v60, v60 row_ror:2 row_mask:0xf bank_mask:0xf\n"
    "v_add_f32_dpp v70, v70, v70 row_ror:2 row_mask:0xf bank_mask:0xf\n"
    "v_add_f32_dpp v62, v62, v62 row_ror:2 row_mask:0xf bank_mask:0xf\n"
    "v_add_f32_dpp v72, v72, v72 row_ror:2 row_mask:0xf bank_mask:0xf\n"
    "v_add_f32_dpp v64, v64, v64 row_ror:2 row_mask:0xf bank_mask:0xf\n"
    "v_add_f32_dpp v74, v74, v74 row_ror:2 row_mask:0xf bank_mask:0xf\n"
    "v_add_f32_dpp v66, v66, v66 row_ror:2 row_mask:0xf bank_mask:0xf\n"
    "v_add_f32_dpp v76, v76, v76 row_ror:2 row_mask:0xf bank_mask:0xf\n"
    "v_add_f32_dpp v68, v68, v68 row_ror:2 row_mask:0xf bank_mask:0xf\n"
    "v_add_f32_dpp v78, v78, v78 row_ror:2 row_mask:0xf bank_mask:0xf\n"
    "v_add_f32_dpp v60, v60, v60 row_ror:4 row_mask:0xf bank_mask:0xf\n"
    "v_add_f32_dpp v70, v70, v70 row_ror:4 row_mask:0xf bank_mask:0xf\n"
    "v_add_f32_dpp v62, v62, v62 row_ror:4 row_mask:0xf bank_mask:0xf\n"
    "v_add_f32_dpp v72, v72, v72 row_ror:4 row_mask:0xf bank_mask:0xf\n"
    "v_add_f32_dpp v64, v64, v64 row_ror:4 row_mask:0xf bank_mask:0xf\n"
    "v_add_f32_dpp v74, v74, v74 row_ror:4 row_mask:0xf bank_mask:0xf\n"
    "v_add_f32_dpp v66, v66, v66 row_ror:4 row_mask:0xf bank_mask:0xf\n"
    "v_add_f32_dpp v76, v76, v76 row_ror:4 row_mask:0xf bank_mask:0xf\n"
    "v_add_f32_dpp v68, v68, v68 row_ror:4 row_mask:0xf bank_mask:0xf\n"
    "v_add_f32_dpp v78, v78, v78 row_ror:4 row_mask:0xf bank_mask:0xf\n"
    "v_add_f32_dpp v60, v60, v60 row_ror:8 row_mask:0xf bank_mask:0xf\n"
    "v_add_f32_dpp v70, v70, v70 row_ror:8 row_mask:0xf bank_mask:0xf\n"
    "v_add_f32_dpp v62, v62, v62 row_ror:8 row_mask:0xf bank_mask:0xf\n"
    "v_add_f32_dpp v72, v72, v72 row_ror:8 row_mask:0xf bank_mask:0xf\n"
    "v_add_f32_dpp v64, v64, v64 row_ror:8 row_mask:0xf bank_mask:0xf\n"
    "s_mov_b32 s11, 0x60000\n"
    "buffer_load_dwordx4 v[40:43], v31, s[40:43], s11 offen sc1\n"
    "buffer_load_dwordx4 v[44:47], v31, s[40:43], s11 offen offset:1024 sc1\n"
    "s_mov_b32 s11, 0x66000\n"
    "buffer_load_dwordx4 v[48:51], v31, s[40:43], s11 offen sc1\n"
    "buffer_load_dwordx4 v[52:55], v31, s[40:43], s11 offen offset:1024 sc1\n"
    "s_mov_b32 s11, 0x6c000\n"
    "buffer_load_dwordx4 v[80:83], v31, s[40:43], s11 offen sc1\n"
    "buffer_load_dwordx4 v[84:87], v31, s[40:43], s11 offen offset:1024 sc1\n"
    "s_mov_b32 s11, 0x72000\n"
    "buffer_load_dwordx4 v[88:91], v31, s[40:43], s11 offen sc1\n"
    "buffer_load_dwordx4 v[92:95], v31, s[40:43], s11 offen offset:1024 sc1\n"
    "v_add_f32_dpp v74, v74, v74 row_ror:8 row_mask:0xf bank_mask:0xf\n"
    "v_add_f32_dpp v66, v66, v66 row_ror:8 row_mask:0xf bank_mask:0xf\n"
    "v_add_f32_dpp v76, v76, v76 row_ror:8 row_mask:0xf bank_mask:0xf\n"
    "v_add_f32_dpp v68, v68, v68 row_ror:8 row_mask:0xf bank_mask:0xf\n"
    "v_add_f32_dpp v78, v78, v78 row_ror:8 row_mask:0xf bank_mask:0xf\n"
    "s_mov_b32 s9, 0x3727c5ac\n"
    "s_mov_b32 s7, 0x3b000000\n"
    "v_mul_f32 v60, s7, v60\n"
    "v_mul_f32 v62, s7, v62\n"
    "v_mul_f32 v64, s7, v64\n"
    "v_mul_f32 v66, s7, v66\n"
    "v_mul_f32 v68, s7, v68\n"
    "v_mul_f32 v61, v60, v60\n"
    "v_mul_f32 v63, v62, v62\n"
    "v_mul_f32 v65, v64, v64\n"
    "v_mul_f32 v67, v66, v66\n"
    "v_mul_f32 v69, v68, v68\n"
    "v_fma_f32 v70, v70, s7, -v61\n"
    "v_fma_f32 v72, v72, s7, -v63\n"
    "v_fma_f32 v74, v74, s7, -v65\n"
    "s_add_u32 m0, s93, 0x20000\n"
    "s_mul_i32 s10, s72, 0x18000\n"
    "buffer_load_dwordx4 v11, s[20:23], s10 offen lds\n"
    "v_fma_f32 v76, v76, s7, -v67\n"
    "v_fma_f32 v78, v78, s7, -v69\n"
    "v_max_f32 v70, 0, v70\n"
    "v_max_f32 v72, 0, v72\n"
    "v_max_f32 v74, 0, v74\n"
    "v_max_f32 v76, 0, v76\n"
    "v_max_f32 v78, 0, v78\n"
    "v_add_f32 v70, s9, v70\n"
    "v_add_f32 v72, s9, v72\n"
    "v_add_f32 v74, s9, v74\n"
    "v_add_f32 v76, s9, v76\n"
    "v_add_f32 v78, s9, v78\n"
    "v_rsq_f32 v70, v70\n"
    "v_rsq_f32 v72, v72\n"
    "v_rsq_f32 v74, v74\n"
    "v_rsq_f32 v76, v76\n"
    "buffer_load_dwordx4 v11, s[20:23], s10 offen offset:1024 lds\n"
    "v_rsq_f32 v78, v78\n"
    "v_mul_f32 v60, v60, v70\n"
    "v_mul_f32 v62, v62, v72\n"
    "v_mul_f32 v64, v64, v74\n"
    "v_mul_f32 v66, v66, v76\n"
    "v_mul_f32 v68, v68, v78\n"
    "v_pk_fma_f32 v[96:97], v[96:97], v[70:71], v[60:61] op_sel_hi:[1,0,0] neg_lo:[0,0,1] neg_hi:[0,0,1]\n"
    "v_pk_fma_f32 v[98:99], v[98:99], v[70:71], v[60:61] op_sel_hi:[1,0,0] neg_lo:[0,0,1] neg_hi:[0,0,1]\n"
    "v_pk_fma_f32 v[100:101], v[100:101], v[70:71], v[60:61] op_sel_hi:[1,0,0] neg_lo:[0,0,1] neg_hi:[0,0,1]\n"
    "v_pk_fma_f32 v[102:103], v[102:103], v[70:71], v[60:61] op_sel_hi:[1,0,0] neg_lo:[0,0,1] neg_hi:[0,0,1]\n"
    "v_pk_fma_f32 v[104:105], v[104:105], v[72:73], v[62:63] op_sel_hi:[1,0,0] neg_lo:[0,0,1] neg_hi:[0,0,1]\n"
    "v_pk_fma_f32 v[106:107], v[106:107], v[72:73], v[62:63] op_sel_hi:[1,0,0] neg_lo:[0,0,1] neg_hi:[0,0,1]\n"
    "v_pk_fma_f32 v[108:109], v[108:109], v[72:73], v[62:63] op_sel_hi:[1,0,0] neg_lo:[0,0,1] neg_hi:[0,0,1]\n"
    "v_pk_fma_f32 v[110:111], v[110:111], v[72:73], v[62:63] op_sel_hi:[1,0,0] neg_lo:[0,0,1] neg_hi:[0,0,1]\n"
    "v_pk_fma_f32 v[112:113], v[112:113], v[74:75], v[64:65] op_sel_hi:[1,0,0] neg_lo:[0,0,1] neg_hi:[0,0,1]\n"
    "v_pk_fma_f32 v[114:115], v[114:115], v[74:75], v[64:65] op_sel_hi:[1,0,0] neg_lo:[0,0,1] neg_hi:[0,0,1]\n"
    "v_pk_fma_f32 v[116:117], v[116:117], v[74:75], v[64:65] op_sel_hi:[1,0,0] neg_lo:[0,0,1] neg_hi:[0,0,1]\n"
    "v_pk_fma_f32 v[118:119], v[118:119], v[74:75], v[64:65] op_sel_hi:[1,0,0] neg_lo:[0,0,1] neg_hi:[0,0,1]\n"
    "v_pk_fma_f32 v[120:121], v[120:121], v[76:77], v[66:67] op_sel_hi:[1,0,0] neg_lo:[0,0,1] neg_hi:[0,0,1]\n"
    "buffer_load_dwordx4 v11, s[20:23], s10 offen offset:2048 lds\n"
    "s_add_u32 m0, s93, 0x23000\n"
    "s_mul_i32 s10, s73, 0x18000\n"
    "buffer_load_dwordx4 v11, s[20:23], s10 offen lds\n"
    "v_pk_fma_f32 v[122:123], v[122:123], v[76:77], v[66:67] op_sel_hi:[1,0,0] neg_lo:[0,0,1] neg_hi:[0,0,1]\n"
    "v_pk_fma_f32 v[124:125], v[124:125], v[76:77], v[66:67] op_sel_hi:[1,0,0] neg_lo:[0,0,1] neg_hi:[0,0,1]\n"
    "v_pk_fma_f32 v[126:127], v[126:127], v[76:77], v[66:67] op_sel_hi:[1,0,0] neg_lo:[0,0,1] neg_hi:[0,0,1]\n"
    "v_pk_fma_f32 v[128:129], v[128:129], v[78:79], v[68:69] op_sel_hi:[1,0,0] neg_lo:[0,0,1] neg_hi:[0,0,1]\n"
    "v_pk_fma_f32 v[130:131], v[130:131], v[78:79], v[68:69] op_sel_hi:[1,0,0] neg_lo:[0,0,1] neg_hi:[0,0,1]\n"
    "v_pk_fma_f32 v[132:133], v[132:133], v[78:79], v[68:69] op_sel_hi:[1,0,0] neg_lo:[0,0,1] neg_hi:[0,0,1]\n"
    "v_pk_fma_f32 v[134:135], v[134:135], v[78:79], v[68:69] op_sel_hi:[1,0,0] neg_lo:[0,0,1] neg_hi:[0,0,1]\n"
    "v_pk_fma_f32 v[136:137], v[136:137], v[70:71], v[60:61] op_sel_hi:[1,0,0] neg_lo:[0,0,1] neg_hi:[0,0,1]\n"
    "v_pk_fma_f32 v[138:139], v[138:139], v[70:71], v[60:61] op_sel_hi:[1,0,0] neg_lo:[0,0,1] neg_hi:[0,0,1]\n"
    "v_pk_fma_f32 v[140:141], v[140:141], v[70:71], v[60:61] op_sel_hi:[1,0,0] neg_lo:[0,0,1] neg_hi:[0,0,1]\n"
    "v_pk_fma_f32 v[142:143], v[142:143], v[70:71], v[60:61] op_sel_hi:[1,0,0] neg_lo:[0,0,1] neg_hi:[0,0,1]\n"
    "v_pk_fma_f32 v[144:145], v[144:145], v[72:73], v[62:63] op_sel_hi:[1,0,0] neg_lo:[0,0,1] neg_hi:[0,0,1]\n"
    "v_pk_fma_f32 v[146:147], v[146:147], v[72:73], v[62:63] op_sel_hi:[1,0,0] neg_lo:[0,0,1] neg_hi:[0,0,1]\n"
    "v_pk_fma_f32 v[148:149], v[148:149], v[72:73], v[62:63] op_sel_hi:[1,0,0] neg_lo:[0,0,1] neg_hi:[0,0,1]\n"
    "v_pk_fma_f32 v[150:151], v[150:151], v[72:73], v[62:63] op_sel_hi:[1,0,0] neg_lo:[0,0,1] neg_hi:[0,0,1]\n"
    "v_pk_fma_f32 v[152:153], v[152:153], v[74:75], v[64:65] op_sel_hi:[1,0,0] neg_lo:[0,0,1] neg_hi:[0,0,1]\n"
    "v_pk_fma_f32 v[154:155], v[154:155], v[74:75], v[64:65] op_sel_hi:[1,0,0] neg_lo:[0,0,1] neg_hi:[0,0,1]\n"
    "v_pk_fma_f32 v[156:157], v[156:157], v[74:75], v[64:65] op_sel_hi:[1,0,0] neg_lo:[0,0,1] neg_hi:[0,0,1]\n"
    "v_pk_fma_f32 v[158:159], v[158:159], v[74:75], v[64:65] op_sel_hi:[1,0,0] neg_lo:[0,0,1] neg_hi:[0,0,1]\n"
    "v_pk_fma_f32 v[160:161], v[160:161], v[76:77], v[66:67] op_sel_hi:[1,0,0] neg_lo:[0,0,1] neg_hi:[0,0,1]\n"
    "buffer_load_dwordx4 v11, s[20:23], s10 offen offset:1024 lds\n"
    "v_pk_fma_f32 v[162:163], v[162:163], v[76:77], v[66:67] op_sel_hi:[1,0,0] neg_lo:[0,0,1] neg_hi:[0,0,1]\n"
    "v_pk_fma_f32 v[164:165], v[164:165], v[76:77], v[66:67] op_sel_hi:[1,0,0] neg_lo:[0,0,1] neg_hi:[0,0,1]\n"
    "v_pk_fma_f32 v[166:167], v[166:167], v[76:77], v[66:67] op_sel_hi:[1,0,0] neg_lo:[0,0,1] neg_hi:[0,0,1]\n"
    "v_pk_fma_f32 v[168:169], v[168:169], v[78:79], v[68:69] op_sel_hi:[1,0,0] neg_lo:[0,0,1] neg_hi:[0,0,1]\n"
    "v_pk_fma_f32 v[170:171], v[170:171], v[78:79], v[68:69] op_sel_hi:[1,0,0] neg_lo:[0,0,1] neg_hi:[0,0,1]\n"
    "v_pk_fma_f32 v[172:173], v[172:173], v[78:79], v[68:69] op_sel_hi:[1,0,0] neg_lo:[0,0,1] neg_hi:[0,0,1]\n"
    "v_pk_fma_f32 v[174:175], v[174:175], v[78:79], v[68:69] op_sel_hi:[1,0,0] neg_lo:[0,0,1] neg_hi:[0,0,1]\n"
    "v_pk_fma_f32 v[176:177], v[176:177], v[70:71], v[60:61] op_sel_hi:[1,0,0] neg_lo:[0,0,1] neg_hi:[0,0,1]\n"
    "v_pk_fma_f32 v[178:179], v[178:179], v[70:71], v[60:61] op_sel_hi:[1,0,0] neg_lo:[0,0,1] neg_hi:[0,0,1]\n"
    "v_pk_fma_f32 v[180:181], v[180:181], v[70:71], v[60:61] op_sel_hi:[1,0,0] neg_lo:[0,0,1] neg_hi:[0,0,1]\n"
    "v_pk_fma_f32 v[182:183], v[182:183], v[70:71], v[60:61] op_sel_hi:[1,0,0] neg_lo:[0,0,1] neg_hi:[0,0,1]\n"
    "v_pk_fma_f32 v[184:185], v[184:185], v[72:73], v[62:63] op_sel_hi:[1,0,0] neg_lo:[0,0,1] neg_hi:[0,0,1]\n"
    "v_pk_fma_f32 v[186:187], v[186:187], v[72:73], v[62:63] op_sel_hi:[1,0,0] neg_lo:[0,0,1] neg_hi:[0,0,1]\n"
    "v_pk_fma_f32 v[188:189], v[188:189], v[72:73], v[62:63] op_sel_hi:[1,0,0] neg_lo:[0,0,1] neg_hi:[0,0,1]\n"
    "v_pk_fma_f32 v[190:191], v[190:191], v[72:73], v[62:63] op_sel_hi:[1,0,0] neg_lo:[0,0,1] neg_hi:[0,0,1]\n"
    "v_pk_fma_f32 v[192:193], v[192:193], v[74:75], v[64:65] op_sel_hi:[1,0,0] neg_lo:[0,0,1] neg_hi:[0,0,1]\n"
    "v_pk_fma_f32 v[194:195], v[194:195], v[74:75], v[64:65] op_sel_hi:[1,0,0] neg_lo:[0,0,1] neg_hi:[0,0,1]\n"
    "v_pk_fma_f32 v[196:197], v[196:197], v[74:75], v[64:65] op_sel_hi:[1,0,0] neg_lo:[0,0,1] neg_hi:[0,0,1]\n"
    "v_pk_fma_f32 v[198:199], v[198:199], v[74:75], v[64:65] op_sel_hi:[1,0,0] neg_lo:[0,0,1] neg_hi:[0,0,1]\n"
    "v_pk_fma_f32 v[200:201], v[200:201], v[76:77], v[66:67] op_sel_hi:[1,0,0] neg_lo:[0,0,1] neg_hi:[0,0,1]\n"
    "buffer_load_dwordx4 v11, s[20:23], s10 offen offset:2048 lds\n"
    "v_pk_fma_f32 v[202:203], v[202:203], v[76:77], v[66:67] op_sel_hi:[1,0,0] neg_lo:[0,0,1] neg_hi:[0,0,1]\n"
    "v_pk_fma_f32 v[204:205], v[204:205], v[76:77], v[66:67] op_sel_hi:[1,0,0] neg_lo:[0,0,1] neg_hi:[0,0,1]\n"
    "v_pk_fma_f32 v[206:207], v[206:207], v[76:77], v[66:67] op_sel_hi:[1,0,0] neg_lo:[0,0,1] neg_hi:[0,0,1]\n"
    "v_pk_fma_f32 v[208:209], v[208:209], v[78:79], v[68:69] op_sel_hi:[1,0,0] neg_lo:[0,0,1] neg_hi:[0,0,1]\n"
    "v_pk_fma_f32 v[210:211], v[210:211], v[78:79], v[68:69] op_sel_hi:[1,0,0] neg_lo:[0,0,1] neg_hi:[0,0,1]\n"
    "v_pk_fma_f32 v[212:213], v[212:213], v[78:79], v[68:69] op_sel_hi:[1,0,0] neg_lo:[0,0,1] neg_hi:[0,0,1]\n"
    "v_pk_fma_f32 v[214:215], v[214:215], v[78:79], v[68:69] op_sel_hi:[1,0,0] neg_lo:[0,0,1] neg_hi:[0,0,1]\n"
    "v_pk_fma_f32 v[216:217], v[216:217], v[70:71], v[60:61] op_sel_hi:[1,0,0] neg_lo:[0,0,1] neg_hi:[0,0,1]\n"
    "v_pk_fma_f32 v[218:219], v[218:219], v[70:71], v[60:61] op_sel_hi:[1,0,0] neg_lo:[0,0,1] neg_hi:[0,0,1]\n"
    "v_pk_fma_f32 v[220:221], v[220:221], v[70:71], v[60:61] op_sel_hi:[1,0,0] neg_lo:[0,0,1] neg_hi:[0,0,1]\n"
    "v_pk_fma_f32 v[222:223], v[222:223], v[70:71], v[60:61] op_sel_hi:[1,0,0] neg_lo:[0,0,1] neg_hi:[0,0,1]\n"
    "v_pk_fma_f32 v[224:225], v[224:225], v[72:73], v[62:63] op_sel_hi:[1,0,0] neg_lo:[0,0,1] neg_hi:[0,0,1]\n"
    "v_pk_fma_f32 v[226:227], v[226:227], v[72:73], v[62:63] op_sel_hi:[1,0,0] neg_lo:[0,0,1] neg_hi:[0,0,1]\n"
    "v_pk_fma_f32 v[228:229], v[228:229], v[72:73], v[62:63] op_sel_hi:[1,0,0] neg_lo:[0,0,1] neg_hi:[0,0,1]\n"
    "v_pk_fma_f32 v[230:231], v[230:231], v[72:73], v[62:63] op_sel_hi:[1,0,0] neg_lo:[0,0,1] neg_hi:[0,0,1]\n"
    "v_pk_fma_f32 v[232:233], v[232:233], v[74:75], v[64:65] op_sel_hi:[1,0,0] neg_lo:[0,0,1] neg_hi:[0,0,1]\n"
    "v_pk_fma_f32 v[234:235], v[234:235], v[74:75], v[64:65] op_sel_hi:[1,0,0] neg_lo:[0,0,1] neg_hi:[0,0,1]\n"
    "v_pk_fma_f32 v[236:237], v[236:237], v[74:75], v[64:65] op_sel_hi:[1,0,0] neg_lo:[0,0,1] neg_hi:[0,0,1]\n"
    "v_pk_fma_f32 v[238:239], v[238:239], v[74:75], v[64:65] op_sel_hi:[1,0,0] neg_lo:[0,0,1] neg_hi:[0,0,1]\n"
    "v_pk_fma_f32 v[240:241], v[240:241], v[76:77], v[66:67] op_sel_hi:[1,0,0] neg_lo:[0,0,1] neg_hi:[0,0,1]\n"
    "v_pk_fma_f32 v[242:243], v[242:243], v[76:77], v[66:67] op_sel_hi:[1,0,0] neg_lo:[0,0,1] neg_hi:[0,0,1]\n"
    "v_pk_fma_f32 v[244:245], v[244:245], v[76:77], v[66:67] op_sel_hi:[1,0,0] neg_lo:[0,0,1] neg_hi:[0,0,1]\n"
    "v_pk_fma_f32 v[246:247], v[246:247], v[76:77], v[66:67] op_sel_hi:[1,0,0] neg_lo:[0,0,1] neg_hi:[0,0,1]\n"
    "v_pk_fma_f32 v[248:249], v[248:249], v[78:79], v[68:69] op_sel_hi:[1,0,0] neg_lo:[0,0,1] neg_hi:[0,0,1]\n"
    "v_pk_fma_f32 v[250:251], v[250:251], v[78:79], v[68:69] op_sel_hi:[1,0,0] neg_lo:[0,0,1] neg_hi:[0,0,1]\n"
    "v_pk_fma_f32 v[252:253], v[252:253], v[78:79], v[68:69] op_sel_hi:[1,0,0] neg_lo:[0,0,1] neg_hi:[0,0,1]\n"
    "v_pk_fma_f32 v[254:255], v[254:255], v[78:79], v[68:69] op_sel_hi:[1,0,0] neg_lo:[0,0,1] neg_hi:[0,0,1]\n"
    "s_waitcnt vmcnt(13)\n"
    "v_xor_b32 v56, s67, v41\n"
    "v_bitop3_b32 v56, v56, v43, s67 bitop3:0xf6\n"
    "s_waitcnt vmcnt(12)\n"
    "v_bitop3_b32 v56, v56, v45, s67 bitop3:0xf6\n"
    "v_bitop3_b32 v56, v56, v47, s67 bitop3:0xf6\n"
    "s_waitcnt vmcnt(11)\n"
    "v_bitop3_b32 v56, v56, v49, s67 bitop3:0xf6\n"
    "v_bitop3_b32 v56, v56, v51, s67 bitop3:0xf6\n"
    "s_waitcnt vmcnt(10)\n"
    "v_bitop3_b32 v56, v56, v53, s67 bitop3:0xf6\n"
    "v_bitop3_b32 v56, v56, v55, s67 bitop3:0xf6\n"
    "s_waitcnt vmcnt(9)\n"
    "v_bitop3_b32 v56, v56, v81, s67 bitop3:0xf6\n"
    "v_bitop3_b32 v56, v56, v83, s67 bitop3:0xf6\n"
    "s_waitcnt vmcnt(8)\n"
    "v_bitop3_b32 v56, v56, v85, s67 bitop3:0xf6\n"
    "v_bitop3_b32 v56, v56, v87, s67 bitop3:0xf6\n"
    "s_waitcnt vmcnt(7)\n"
    "v_bitop3_b32 v56, v56, v89, s67 bitop3:0xf6\n"
    "v_bitop3_b32 v56, v56, v91, s67 bitop3:0xf6\n"
    "s_waitcnt vmcnt(6)\n"
    "v_bitop3_b32 v56, v56, v93, s67 bitop3:0xf6\n"
    "v_bitop3_b32 v56, v56, v95, s67 bitop3:0xf6\n"
    "v_cmp_ne_u32 vcc, 0, v56\n"
    "s_nop 0\n"
    "s_cbranch_vccnz L_g1\n"
    "L_g1_ok:\n"
    "v_add_f32 v60, v40, v48\n"
    "v_add_f32 v80, v80, v88\n"
    "v_add_f32 v61, v42, v50\n"
    "v_add_f32 v82, v82, v90\n"
    "v_add_f32 v62, v44, v52\n"
    "v_add_f32 v84, v84, v92\n"
    "v_add_f32 v63, v46, v54\n"
    "v_add_f32 v86, v86, v94\n"
    "v_add_f32 v60, v60, v80\n"
    "v_add_f32 v61, v61, v82\n"
    "v_add_f32 v62, v62, v84\n"
    "v_add_f32 v63, v63, v86\n"
    "s_cmp_ge_u32 s6, 2\n"
    "s_cselect_b32 s7, 1.0, 0\n"
    "v_cvt_pk_bf16_f32 v40, v60, v61\n"
    "v_cvt_pk_bf16_f32 v41, v62, v63\n"
    "v_lshlrev_b32 v60, 16, v40\n"
    "v_and_b32 v61, 0xffff0000, v40\n"
    "v_lshlrev_b32 v62, 16, v41\n"
    "v_and_b32 v63, 0xffff0000, v41\n"
    "v_add_f32 v60, s7, v60\n"
    "v_add_f32 v61, s7, v61\n"
    "v_add_f32 v62, s7, v62\n"
    "v_add_f32 v63, s7, v63\n"
    "v_cvt_pk_bf16_f32 v40, v60, v61\n"
    "v_cvt_pk_bf16_f32 v41, v62, v63\n"
    "v_lshlrev_b32 v60, 16, v40\n"
    "v_and_b32 v61, 0xffff0000, v40\n"
    "v_lshlrev_b32 v62, 16, v41\n"
    "v_and_b32 v63, 0xffff0000, v41\n"
    "ds_write_b64 v23, v[60:61]\n"
    "ds_write_b64 v38, v[62:63]\n"
    "s_waitcnt lgkmcnt(0)\n"
    "s_barrier\n"
    "ds_read_b64_tr_b16 a[0:1], v9 offset:16384\n"
    "ds_read_b64_tr_b16 a[2:3], v9 offset:16896\n"
    "ds_read_b64_tr_b16 a[4:5], v9 offset:17408\n"
    "ds_read_b64_tr_b16 a[6:7], v9 offset:17920\n"
    "ds_read_b64_tr_b16 a[8:9], v9 offset:18432\n"
    "ds_read_b64_tr_b16 a[10:11], v9 offset:18944\n"
    "ds_read_b64_tr_b16 a[12:13], v9 offset:28672\n"
    "ds_read_b64_tr_b16 a[14:15], v9 offset:29184\n"
    "ds_read_b64_tr_b16 a[16:17], v9 offset:29696\n"
    "ds_read_b64_tr_b16 a[18:19], v9 offset:30208\n"
    "ds_read_b64_tr_b16 a[20:21], v9 offset:30720\n"
    "ds_read_b64_tr_b16 a[22:23], v9 offset:31232\n"
    "ds_read_b64_tr_b16 a[24:25], v9 offset:40960\n"
    "ds_read_b64_tr_b16 a[26:27], v9 offset:41472\n"
    "ds_read_b64_tr_b16 a[28:29], v9 offset:41984\n"
    "ds_read_b64_tr_b16 a[30:31], v9 offset:42496\n"
    "ds_read_b64_tr_b16 a[32:33], v9 offset:43008\n"
    "ds_read_b64_tr_b16 a[34:35], v9 offset:43520\n"
    "ds_read_b64_tr_b16 a[36:37], v9 offset:53248\n"
    "ds_read_b64_tr_b16 a[38:39], v9 offset:53760\n"
    "ds_read_b64_tr_b16 a[40:41], v9 offset:54272\n"
    "ds_read_b64_tr_b16 a[42:43], v9 offset:54784\n"
    "ds_read_b64_tr_b16 a[44:45], v9 offset:55296\n"
    "ds_read_b64_tr_b16 a[46:47], v9 offset:55808\n"
    "ds_read_b128 v[40:43], v12 offset:2048\n"
    "ds_read_b128 v[48:51], v12\n"
    "ds_read_b128 v[44:47], v12 offset:2304\n"
    "ds_read_b128 v[52:55], v12 offset:256\n"
    "ds_read_b128 v[56:59], v12 offset:2560\n"
    "ds_read_b128 v[64:67], v12 offset:512\n"
    "ds_read_b128 v[60:63], v12 offset:2816\n"
    "ds_read_b128 v[68:71], v12 offset:768\n"
    "s_waitcnt lgkmcnt(8)\n"
    "s_barrier\n"
    "s_waitcnt lgkmcnt(6)\n"
    "v_pk_fma_f32 v[96:97], v[96:97], v[40:41], v[48:49]\n"
    "v_pk_fma_f32 v[98:99], v[98:99], v[42:43], v[50:51]\n"
    "s_waitcnt lgkmcnt(4)\n"
    "v_pk_fma_f32 v[100:101], v[100:101], v[44:45], v[52:53]\n"
    "v_pk_fma_f32 v[102:103], v[102:103], v[46:47], v[54:55]\n"
    "v_cvt_pk_bf16_f32 v96, v96, v97\n"
    "s_lshl_b32 s7, s6, 4\n"
    "v_lshrrev_b32 v88, 2, v0\n"
    "v_add_u32 v88, s7, v88\n"
    "v_mul_u32_u24 v88, 0x1800, v88\n"
    "v_and_b32 v89, 3, v0\n"
    "v_lshl_add_u32 v88, v89, 4, v88\n"
    "s_lshl_b32 s7, s94, 6\n"
    "s_add_u32 s7, s7, s95\n"
    "s_add_u32 s8, s7, 0x60000\n"
    "buffer_load_dwordx4 a[192:195], v88, s[36:39], s7 offen offset:2048\n"
    "buffer_load_dwordx4 a[196:199], v88, s[36:39], s8 offen offset:2048\n"
    "s_lshl_b32 s11, s2, 2\n"
    "s_add_u32 s11, s11, s6\n"
    "s_mul_i32 s11, s11, 11\n"
    "v_min_u32 v89, 10, v0\n"
    "v_add_u32 v89, s11, v89\n"
    "v_lshlrev_b32 v89, 7, v89\n"
    "buffer_load_dword v90, v89, s[28:31], 0 offen\n"
    "v_bfe_u32 v92, v0, 1, 3\n"
    "v_and_b32 v93, 1, v0\n"
    "v_lshlrev_b32 v93, 4, v93\n"
    "s_lshl_b32 s9, s63, 8\n"
    "v_add_u32 v93, s9, v93\n"
    "s_lshl_b32 s9, s6, 3\n"
    "v_add_u32 v91, 0, v14\n"
    "v_xor_b32 v94, v91, v92\n"
    "v_lshl_add_u32 v94, v94, 5, v93\n"
    "v_add_u32 v91, s9, v91\n"
    "v_mul_u32_u24 v91, 0xac0, v91\n"
    "v_add_u32 v95, v91, v94\n"
    "v_add_u32 v91, 4, v14\n"
    "v_xor_b32 v94, v91, v92\n"
    "v_lshl_add_u32 v94, v94, 5, v93\n"
    "v_add_u32 v91, s9, v91\n"
    "v_mul_u32_u24 v91, 0xac0, v91\n"
    "v_add_u32 v87, v91, v94\n"
    "v_subrev_u32 v87, 0x400, v87\n"
    "v_cvt_pk_bf16_f32 v97, v98, v99\n"
    "v_cvt_pk_bf16_f32 v98, v100, v101\n"
    "v_cvt_pk_bf16_f32 v99, v102, v103\n"
    "ds_write_b128 v2, v[96:99]\n"
    "v_pk_fma_f32 v[104:105], v[104:105], v[40:41], v[48:49]\n"
    "v_pk_fma_f32 v[106:107], v[106:107], v[42:43], v[50:51]\n"
    "v_pk_fma_f32 v[108:109], v[108:109], v[44:45], v[52:53]\n"
    "v_pk_fma_f32 v[110:111], v[110:111], v[46:47], v[54:55]\n"
    "v_cvt_pk_bf16_f32 v104, v104, v105\n"
    "v_cvt_pk_bf16_f32 v105, v106, v107\n"
    "v_cvt_pk_bf16_f32 v106, v108, v109\n"
    "v_cvt_pk_bf16_f32 v107, v110, v111\n"
    "ds_write_b128 v2, v[104:107] offset:4096\n"
    "v_pk_fma_f32 v[112:113], v[112:113], v[40:41], v[48:49]\n"
    "v_pk_fma_f32 v[114:115], v[114:115], v[42:43], v[50:51]\n"
    "v_pk_fma_f32 v[116:117], v[116:117], v[44:45], v[52:53]\n"
    "v_pk_fma_f32 v[118:119], v[118:119], v[46:47], v[54:55]\n"
    "v_cvt_pk_bf16_f32 v112, v112, v113\n"
    "v_cvt_pk_bf16_f32 v113, v114, v115\n"
    "v_cvt_pk_bf16_f32 v114, v116, v117\n"
    "v_cvt_pk_bf16_f32 v115, v118, v119\n"
    "ds_write_b128 v2, v[112:115] offset:8192\n"
    "v_pk_fma_f32 v[120:121], v[120:121], v[40:41], v[48:49]\n"
    "v_pk_fma_f32 v[122:123], v[122:123], v[42:43], v[50:51]\n"
    "v_pk_fma_f32 v[124:125], v[124:125], v[44:45], v[52:53]\n"
    "s_add_u32 m0, s93, 0x14000\n"
    "s_mul_i32 s10, s74, 0x18000\n"
    "buffer_load_dwordx4 v11, s[20:23], s10 offen lds\n"
    "v_pk_fma_f32 v[126:127], v[126:127], v[46:47], v[54:55]\n"
    "v_cvt_pk_bf16_f32 v120, v120, v121\n"
    "v_cvt_pk_bf16_f32 v121, v122, v123\n"
    "v_cvt_pk_bf16_f32 v122, v124, v125\n"
    "v_cvt_pk_bf16_f32 v123, v126, v127\n"
    "ds_write_b128 v2, v[120:123] offset:12288\n"
    "v_pk_fma_f32 v[128:129], v[128:129], v[40:41], v[48:49]\n"
    "v_pk_fma_f32 v[130:131], v[130:131], v[42:43], v[50:51]\n"
    "v_pk_fma_f32 v[132:133], v[132:133], v[44:45], v[52:53]\n"
    "v_pk_fma_f32 v[134:135], v[134:135], v[46:47], v[54:55]\n"
    "v_cvt_pk_bf16_f32 v128, v128, v129\n"
    "v_cvt_pk_bf16_f32 v129, v130, v131\n"
    "v_cvt_pk_bf16_f32 v130, v132, v133\n"
    "buffer_load_dwordx4 v11, s[20:23], s10 offen offset:1024 lds\n"
    "v_cvt_pk_bf16_f32 v131, v134, v135\n"
    "ds_write_b128 v2, v[128:131] offset:16384\n"
    "v_mov_b32 v112, v95\n"
    "v_mov_b32 v113, v87\n"
    "ds_read_b128 v[40:43], v12 offset:3072\n"
    "ds_read_b128 v[48:51], v12 offset:1024\n"
    "ds_read_b128 v[44:47], v12 offset:3328\n"
    "ds_read_b128 v[52:55], v12 offset:1280\n"
    "s_waitcnt lgkmcnt(11)\n"
    "v_pk_fma_f32 v[136:137], v[136:137], v[56:57], v[64:65]\n"
    "v_pk_fma_f32 v[138:139], v[138:139], v[58:59], v[66:67]\n"
    "s_waitcnt lgkmcnt(9)\n"
    "v_pk_fma_f32 v[140:141], v[140:141], v[60:61], v[68:69]\n"
    "v_pk_fma_f32 v[142:143], v[142:143], v[62:63], v[70:71]\n"
    "v_cvt_pk_bf16_f32 v136, v136, v137\n"
    "buffer_load_dwordx4 v11, s[20:23], s10 offen offset:2048 lds\n"
    "v_cvt_pk_bf16_f32 v137, v138, v139\n"
    "v_cvt_pk_bf16_f32 v138, v140, v141\n"
    "v_cvt_pk_bf16_f32 v139, v142, v143\n"
    "ds_write_b128 v2, v[136:139] offset:20480\n"
    "v_pk_fma_f32 v[144:145], v[144:145], v[56:57], v[64:65]\n"
    "v_pk_fma_f32 v[146:147], v[146:147], v[58:59], v[66:67]\n"
    "v_pk_fma_f32 v[148:149], v[148:149], v[60:61], v[68:69]\n"
    "v_pk_fma_f32 v[150:151], v[150:151], v[62:63], v[70:71]\n"
    "v_cvt_pk_bf16_f32 v144, v144, v145\n"
    "v_cvt_pk_bf16_f32 v145, v146, v147\n"
    "v_cvt_pk_bf16_f32 v146, v148, v149\n"
    "v_cvt_pk_bf16_f32 v147, v150, v151\n"
    "ds_write_b128 v2, v[144:147] offset:24576\n"
    "s_add_u32 m0, s93, 0x17000\n"
    "s_mul_i32 s10, s75, 0x18000\n"
    "buffer_load_dwordx4 v11, s[20:23], s10 offen lds\n"
    "v_pk_fma_f32 v[152:153], v[152:153], v[56:57], v[64:65]\n"
    "v_pk_fma_f32 v[154:155], v[154:155], v[58:59], v[66:67]\n"
    "v_pk_fma_f32 v[156:157], v[156:157], v[60:61], v[68:69]\n"
    "v_pk_fma_f32 v[158:159], v[158:159], v[62:63], v[70:71]\n"
    "v_cvt_pk_bf16_f32 v152, v152, v153\n"
    "v_cvt_pk_bf16_f32 v153, v154, v155\n"
    "v_cvt_pk_bf16_f32 v154, v156, v157\n"
    "v_cvt_pk_bf16_f32 v155, v158, v159\n"
    "ds_write_b128 v2, v[152:155] offset:28672\n"
    "v_pk_fma_f32 v[160:161], v[160:161], v[56:57], v[64:65]\n"
    "v_pk_fma_f32 v[162:163], v[162:163], v[58:59], v[66:67]\n"
    "v_pk_fma_f32 v[164:165], v[164:165], v[60:61], v[68:69]\n"
    "v_pk_fma_f32 v[166:167], v[166:167], v[62:63], v[70:71]\n"
    "buffer_load_dwordx4 v11, s[20:23], s10 offen offset:1024 lds\n"
    "v_cvt_pk_bf16_f32 v160, v160, v161\n"
    "v_cvt_pk_bf16_f32 v161, v162, v163\n"
    "v_cvt_pk_bf16_f32 v162, v164, v165\n"
    "v_cvt_pk_bf16_f32 v163, v166, v167\n"
    "ds_write_b128 v2, v[160:163] offset:32768\n"
    "v_pk_fma_f32 v[168:169], v[168:169], v[56:57], v[64:65]\n"
    "v_pk_fma_f32 v[170:171], v[170:171], v[58:59], v[66:67]\n"
    "v_pk_fma_f32 v[172:173], v[172:173], v[60:61], v[68:69]\n"
    "v_pk_fma_f32 v[174:175], v[174:175], v[62:63], v[70:71]\n"
    "v_cvt_pk_bf16_f32 v168, v168, v169\n"
    "v_cvt_pk_bf16_f32 v169, v170, v171\n"
    "v_cvt_pk_bf16_f32 v170, v172, v173\n"
    "v_cvt_pk_bf16_f32 v171, v174, v175\n"
    "buffer_load_dwordx4 v11, s[20:23], s10 offen offset:2048 lds\n"
    "ds_write_b128 v2, v[168:171] offset:36864\n"
    "ds_read_b128 v[56:59], v12 offset:3584\n"
    "ds_read_b128 v[64:67], v12 offset:1536\n"
    "ds_read_b128 v[60:63], v12 offset:3840\n"
    "ds_read_b128 v[68:71], v12 offset:1792\n"
    "s_waitcnt lgkmcnt(11)\n"
    "v_pk_fma_f32 v[176:177], v[176:177], v[40:41], v[48:49]\n"
    "v_pk_fma_f32 v[178:179], v[178:179], v[42:43], v[50:51]\n"
    "s_waitcnt lgkmcnt(9)\n"
    "v_pk_fma_f32 v[180:181], v[180:181], v[44:45], v[52:53]\n"
    "v_pk_fma_f32 v[182:183], v[182:183], v[46:47], v[54:55]\n"
    "v_cvt_pk_bf16_f32 v176, v176, v177\n"
    "v_cvt_pk_bf16_f32 v177, v178, v179\n"
    "v_cvt_pk_bf16_f32 v178, v180, v181\n"
    "v_cvt_pk_bf16_f32 v179, v182, v183\n"
    "s_add_u32 m0, s93, 0x1a000\n"
    "s_mul_i32 s10, s76, 0x18000\n"
    "buffer_load_dwordx4 v11, s[20:23], s10 offen lds\n"
    "ds_write_b128 v2, v[176:179] offset:40960\n"
    "v_pk_fma_f32 v[184:185], v[184:185], v[40:41], v[48:49]\n"
    "v_pk_fma_f32 v[186:187], v[186:187], v[42:43], v[50:51]\n"
    "v_pk_fma_f32 v[188:189], v[188:189], v[44:45], v[52:53]\n"
    "v_pk_fma_f32 v[190:191], v[190:191], v[46:47], v[54:55]\n"
    "v_cvt_pk_bf16_f32 v184, v184, v185\n"
    "v_cvt_pk_bf16_f32 v185, v186, v187\n"
    "v_cvt_pk_bf16_f32 v186, v188, v189\n"
    "v_cvt_pk_bf16_f32 v187, v190, v191\n"
    "ds_write_b128 v2, v[184:187] offset:45056\n"
    "v_pk_fma_f32 v[192:193], v[192:193], v[40:41], v[48:49]\n"
    "v_pk_fma_f32 v[194:195], v[194:195], v[42:43], v[50:51]\n"
    "v_pk_fma_f32 v[196:197], v[196:197], v[44:45], v[52:53]\n"
    "buffer_load_dwordx4 v11, s[20:23], s10 offen offset:1024 lds\n"
    "v_pk_fma_f32 v[198:199], v[198:199], v[46:47], v[54:55]\n"
    "v_cvt_pk_bf16_f32 v192, v192, v193\n"
    "v_cvt_pk_bf16_f32 v193, v194, v195\n"
    "v_cvt_pk_bf16_f32 v194, v196, v197\n"
    "v_cvt_pk_bf16_f32 v195, v198, v199\n"
    "ds_write_b128 v2, v[192:195] offset:49152\n"
    "v_pk_fma_f32 v[200:201], v[200:201], v[40:41], v[48:49]\n"
    "v_pk_fma_f32 v[202:203], v[202:203], v[42:43], v[50:51]\n"
    "v_pk_fma_f32 v[204:205], v[204:205], v[44:45], v[52:53]\n"
    "v_pk_fma_f32 v[206:207], v[206:207], v[46:47], v[54:55]\n"
    "v_cvt_pk_bf16_f32 v200, v200, v201\n"
    "v_cvt_pk_bf16_f32 v201, v202, v203\n"
    "v_cvt_pk_bf16_f32 v202, v204, v205\n"
    "buffer_load_dwordx4 v11, s[20:23], s10 offen offset:2048 lds\n"
    "v_cvt_pk_bf16_f32 v203, v206, v207\n"
    "ds_write_b128 v2, v[200:203] offset:53248\n"
    "v_pk_fma_f32 v[208:209], v[208:209], v[40:41], v[48:49]\n"
    "v_pk_fma_f32 v[210:211], v[210:211], v[42:43], v[50:51]\n"
    "v_pk_fma_f32 v[212:213], v[212:213], v[44:45], v[52:53]\n"
    "v_pk_fma_f32 v[214:215], v[214:215], v[46:47], v[54:55]\n"
    "v_cvt_pk_bf16_f32 v208, v208, v209\n"
    "v_cvt_pk_bf16_f32 v209, v210, v211\n"
    "v_cvt_pk_bf16_f32 v210, v212, v213\n"
    "v_cvt_pk_bf16_f32 v211, v214, v215\n"
    "ds_write_b128 v2, v[208:211] offset:57344\n"
    "s_waitcnt lgkmcnt(7)\n"
    "v_pk_fma_f32 v[216:217], v[216:217], v[56:57], v[64:65]\n"
    "v_pk_fma_f32 v[218:219], v[218:219], v[58:59], v[66:67]\n"
    "s_add_u32 m0, s93, 0x1d000\n"
    "s_mul_i32 s10, s77, 0x18000\n"
    "buffer_load_dwordx4 v11, s[20:23], s10 offen lds\n"
    "s_waitcnt lgkmcnt(5)\n"
    "v_pk_fma_f32 v[220:221], v[220:221], v[60:61], v[68:69]\n"
    "v_pk_fma_f32 v[222:223], v[222:223], v[62:63], v[70:71]\n"
    "v_cvt_pk_bf16_f32 v216, v216, v217\n"
    "s_mov_b32 s11, 0x81000\n"
    "buffer_load_dwordx4 v[176:179], v112, s[28:31], s11 offen\n"
    "v_cvt_pk_bf16_f32 v217, v218, v219\n"
    "v_cvt_pk_bf16_f32 v218, v220, v221\n"
    "v_cvt_pk_bf16_f32 v219, v222, v223\n"
    "ds_write_b128 v3, v[216:219]\n"
    "v_pk_fma_f32 v[224:225], v[224:225], v[56:57], v[64:65]\n"
    "v_pk_fma_f32 v[226:227], v[226:227], v[58:59], v[66:67]\n"
    "buffer_load_dwordx4 v[180:183], v113, s[28:31], s11 offen offset:1024\n"
    "v_pk_fma_f32 v[228:229], v[228:229], v[60:61], v[68:69]\n"
    "v_pk_fma_f32 v[230:231], v[230:231], v[62:63], v[70:71]\n"
    "v_cvt_pk_bf16_f32 v224, v224, v225\n"
    "v_cvt_pk_bf16_f32 v225, v226, v227\n"
    "buffer_load_dwordx4 v11, s[20:23], s10 offen offset:1024 lds\n"
    "v_cvt_pk_bf16_f32 v226, v228, v229\n"
    "v_cvt_pk_bf16_f32 v227, v230, v231\n"
    "s_mov_b32 s11, 0x96800\n"
    "buffer_load_dwordx4 v[184:187], v112, s[28:31], s11 offen\n"
    "ds_write_b128 v3, v[224:227] offset:4096\n"
    "v_pk_fma_f32 v[232:233], v[232:233], v[56:57], v[64:65]\n"
    "v_pk_fma_f32 v[234:235], v[234:235], v[58:59], v[66:67]\n"
    "v_pk_fma_f32 v[236:237], v[236:237], v[60:61], v[68:69]\n"
    "v_pk_fma_f32 v[238:239], v[238:239], v[62:63], v[70:71]\n"
    "v_cvt_pk_bf16_f32 v232, v232, v233\n"
    "buffer_load_dwordx4 v[188:191], v113, s[28:31], s11 offen offset:1024\n"
    "v_cvt_pk_bf16_f32 v233, v234, v235\n"
    "v_cvt_pk_bf16_f32 v234, v236, v237\n"
    "v_cvt_pk_bf16_f32 v235, v238, v239\n"
    "ds_write_b128 v3, v[232:235] offset:8192\n"
    "v_pk_fma_f32 v[240:241], v[240:241], v[56:57], v[64:65]\n"
    "buffer_load_dwordx4 v11, s[20:23], s10 offen offset:2048 lds\n"
    "v_pk_fma_f32 v[242:243], v[242:243], v[58:59], v[66:67]\n"
    "s_mov_b32 s11, 0xac000\n"
    "buffer_load_dwordx4 v[192:195], v112, s[28:31], s11 offen\n"
    "v_pk_fma_f32 v[244:245], v[244:245], v[60:61], v[68:69]\n"
    "v_pk_fma_f32 v[246:247], v[246:247], v[62:63], v[70:71]\n"
    "v_cvt_pk_bf16_f32 v240, v240, v241\n"
    "v_cvt_pk_bf16_f32 v241, v242, v243\n"
    "v_cvt_pk_bf16_f32 v242, v244, v245\n"
    "v_cvt_pk_bf16_f32 v243, v246, v247\n"
    "buffer_load_dwordx4 v[196:199], v113, s[28:31], s11 offen offset:1024\n"
    "ds_write_b128 v3, v[240:243] offset:12288\n"
    "v_pk_fma_f32 v[248:249], v[248:249], v[56:57], v[64:65]\n"
    "v_pk_fma_f32 v[250:251], v[250:251], v[58:59], v[66:67]\n"
    "v_pk_fma_f32 v[252:253], v[252:253], v[60:61], v[68:69]\n"
    "v_pk_fma_f32 v[254:255], v[254:255], v[62:63], v[70:71]\n"
    "v_cvt_pk_bf16_f32 v248, v248, v249\n"
    "v_cvt_pk_bf16_f32 v249, v250, v251\n"
    "v_cvt_pk_bf16_f32 v250, v252, v253\n"
    "v_cvt_pk_bf16_f32 v251, v254, v255\n"
    "ds_write_b128 v3, v[248:251] offset:16384\n"
    "s_waitcnt vmcnt(21)\n"
    "s_waitcnt lgkmcnt(0)\n"
    "s_barrier\n"
    "ds_read_b128 v[16:19], v4\n"
    "ds_read_b128 v[20:23], v4 offset:4096\n"
    "ds_read_b128 v[24:27], v4 offset:8192\n"
    "ds_read_b128 v[28:31], v4 offset:12288\n"
    "ds_read_b128 v[32:35], v4 offset:16384\n"
    "ds_read_b128 v[36:39], v5 offset:1024\n"
    "ds_read_b128 v[128:131], v5 offset:5120\n"
    "ds_read_b128 v[132:135], v5 offset:9216\n"
    "s_waitcnt lgkmcnt(5)\n"
    "v_mfma_f32_16x16x32_bf16 v[56:59], a[4:7], v[16:19], 0\n"
    "s_mov_b32 s11, 0xec800\n"
    "buffer_load_dwordx4 v[216:219], v112, s[28:31], s11 offen\n"
    "v_mfma_f32_16x16x32_bf16 v[76:79], v[16:19], a[8:11], 0\n"
    "ds_read_b128 v[16:19], v5 offset:13312\n"
    "v_mfma_f32_16x16x32_bf16 v[60:63], a[4:7], v[20:23], 0\n"
    "ds_read_b64_tr_b16 a[48:49], v10\n"
    "buffer_load_dwordx4 v[220:223], v113, s[28:31], s11 offen offset:1024\n"
    "s_mov_b32 s11, 0x102000\n"
    "v_mfma_f32_16x16x32_bf16 v[80:83], v[20:23], a[8:11], 0\n"
    "ds_read_b64_tr_b16 a[50:51], v10 offset:512\n"
    "v_mfma_f32_16x16x32_bf16 v[40:43], a[0:3], v[20:23], 0\n"
    "buffer_load_dwordx4 v[224:227], v112, s[28:31], s11 offen\n"
    "ds_read_b128 v[20:23], v5 offset:17408\n"
    "v_mfma_f32_16x16x32_bf16 v[64:67], a[4:7], v[24:27], 0\n"
    "ds_read_b64_tr_b16 a[52:53], v10 offset:1024\n"
    "v_mfma_f32_16x16x32_bf16 v[84:87], v[24:27], a[8:11], 0\n"
    "ds_read_b64_tr_b16 a[54:55], v10 offset:1536\n"
    "buffer_load_dwordx4 v[228:231], v113, s[28:31], s11 offen offset:1024\n"
    "s_mov_b32 s11, 0x117800\n"
    "v_mfma_f32_16x16x32_bf16 v[44:47], a[0:3], v[24:27], 0\n"
    "ds_read_b128 v[24:27], v4 offset:2048\n"
    "s_waitcnt lgkmcnt(9)\n"
    "v_mfma_f32_16x16x32_bf16 v[68:71], a[4:7], v[28:31], 0\n"
    "ds_read_b64_tr_b16 a[56:57], v10 offset:2048\n"
    "buffer_load_dwordx4 v[232:235], v112, s[28:31], s11 offen\n"
    "s_waitcnt vmcnt(23)\n"
    "v_mfma_f32_16x16x32_bf16 v[88:91], v[28:31], a[8:11], 0\n"
    "ds_read_b64_tr_b16 a[58:59], v10 offset:2560\n"
    "v_mfma_f32_16x16x32_bf16 v[48:51], a[0:3], v[28:31], 0\n"
    "buffer_load_dwordx4 v[236:239], v113, s[28:31], s11 offen offset:1024\n"
    "s_mov_b32 s11, 0x12d000\n"
    "ds_read_b128 v[28:31], v4 offset:6144\n"
    "v_mfma_f32_16x16x32_bf16 v[72:75], a[4:7], v[32:35], 0\n"
    "ds_read_b64_tr_b16 a[60:61], v10 offset:12288\n"
    "v_mfma_f32_16x16x32_bf16 v[92:95], v[32:35], a[8:11], 0\n"
    "ds_read_b64_tr_b16 a[62:63], v10 offset:12800\n"
    "buffer_load_dwordx4 v[240:243], v112, s[28:31], s11 offen\n"
    "v_mfma_f32_16x16x32_bf16 v[52:55], a[0:3], v[32:35], 0\n"
    "ds_read_b128 v[32:35], v4 offset:10240\n"
    "v_mfma_f32_16x16x32_bf16 v[56:59], a[16:19], v[36:39], v[56:59]\n"
    "ds_read_b64_tr_b16 a[64:65], v10 offset:13312\n"
    "buffer_load_dwordx4 v[244:247], v113, s[28:31], s11 offen offset:1024\n"
    "s_mov_b32 s11, 0x142800\n"
    "v_mfma_f32_16x16x32_bf16 v[76:79], v[36:39], a[20:23], v[76:79]\n"
    "ds_read_b128 v[36:39], v4 offset:14336\n"
    "s_waitcnt lgkmcnt(14)\n"
    "v_mfma_f32_16x16x32_bf16 v[60:63], a[16:19], v[128:131], v[60:63]\n"
    "ds_read_b64_tr_b16 a[66:67], v10 offset:13824\n"
    "buffer_load_dwordx4 v[248:251], v112, s[28:31], s11 offen\n"
    "v_mfma_f32_16x16x32_bf16 v[80:83], v[128:131], a[20:23], v[80:83]\n"
    "ds_read_b64_tr_b16 a[68:69], v10 offset:14336\n"
    "v_mfma_f32_16x16x32_bf16 v[40:43], a[12:15], v[128:131], v[40:43]\n"
    "buffer_load_dwordx4 v[252:255], v113, s[28:31], s11 offen offset:1024\n"
    "v_lshrrev_b32 v138, 3, v0\n"
    "ds_read_b128 v[128:131], v4 offset:18432\n"
    "v_mfma_f32_16x16x32_bf16 v[64:67], a[16:19], v[132:135], v[64:67]\n"
    "ds_read_b64_tr_b16 a[70:71], v10 offset:14848\n"
    "v_lshlrev_b32 v136, 10, v138\n"
    "v_mfma_f32_16x16x32_bf16 v[84:87], v[132:135], a[20:23], v[84:87]\n"
    "v_lshrrev_b32 v138, 1, v138\n"
    "v_bfe_u32 v139, v0, 1, 2\n"
    "v_mfma_f32_16x16x32_bf16 v[44:47], a[12:15], v[132:135], v[44:47]\n"
    "v_xor_b32 v138, v138, v139\n"
    "v_lshl_add_u32 v136, v138, 5, v136\n"
    "ds_read_b128 v[132:135], v5 offset:3072\n"
    "v_mfma_f32_16x16x32_bf16 v[68:71], a[16:19], v[16:19], v[68:71]\n"
    "v_and_b32 v138, 1, v0\n"
    "v_mfma_f32_16x16x32_bf16 v[88:91], v[16:19], a[20:23], v[88:91]\n"
    "v_lshl_add_u32 v136, v138, 4, v136\n"
    "s_lshl_b32 s7, s63, 7\n"
    "v_mfma_f32_16x16x32_bf16 v[48:51], a[12:15], v[16:19], v[48:51]\n"
    "v_add_u32 v136, s7, v136\n"
    "ds_read_b128 v[16:19], v5 offset:7168\n"
    "s_waitcnt lgkmcnt(11)\n"
    "v_mfma_f32_16x16x32_bf16 v[72:75], a[16:19], v[20:23], v[72:75]\n"
    "s_lshl_b32 s7, s6, 17\n"
    "v_add_u32 v136, s7, v136\n"
    "v_mfma_f32_16x16x32_bf16 v[92:95], v[20:23], a[20:23], v[92:95]\n"
    "s_mul_i32 s97, s6, 0x1100\n"
    "v_and_b32 v140, 15, v0\n"
    "v_mfma_f32_16x16x32_bf16 v[52:55], a[12:15], v[20:23], v[52:55]\n"
    "v_lshlrev_b32 v141, 10, v140\n"
    "ds_read_b128 v[20:23], v5 offset:11264\n"
    "v_mfma_f32_16x16x32_bf16 v[56:59], a[28:31], v[24:27], v[56:59]\n"
    "v_and_b32 v142, 1, v14\n"
    "v_lshl_add_u32 v141, v142, 5, v141\n"
    "v_mfma_f32_16x16x32_bf16 v[76:79], v[24:27], a[32:35], v[76:79]\n"
    "v_lshrrev_b32 v142, 1, v14\n"
    "ds_read_b128 v[24:27], v5 offset:15360\n"
    "v_mfma_f32_16x16x32_bf16 v[60:63], a[28:31], v[28:31], v[60:63]\n"
    "v_lshl_add_u32 v143, v142, 4, v141\n"
    "s_lshl_b32 s7, s62, 16\n"
    "v_mfma_f32_16x16x32_bf16 v[80:83], v[28:31], a[32:35], v[80:83]\n"
    "s_lshl_b32 s8, s87, 14\n"
    "s_add_u32 s7, s7, s8\n"
    "s_lshl_b32 s8, s63, 7\n"
    "s_add_u32 s84, s7, s8\n"
    "buffer_load_dwordx4 a[200:203], v143, s[16:19], s84 offen\n"
    "buffer_load_dwordx4 a[204:207], v143, s[16:19], s84 offen offset:64\n"
    "v_lshlrev_b32 v141, 3, v140\n"
    "buffer_load_dwordx2 a[208:209], v141, s[52:55], s8 offen\n"
    "v_mfma_f32_16x16x32_bf16 v[40:43], a[24:27], v[28:31], v[40:43]\n"
    "s_waitcnt vmcnt(25)\n"
    "s_waitcnt lgkmcnt(4)\n"
    "s_barrier\n"
    "ds_read_b128 v[28:31], v5 offset:19456\n"
    "v_mfma_f32_16x16x32_bf16 v[64:67], a[28:31], v[32:35], v[64:67]\n"
    "s_add_u32 m0, s93, 0x20000\n"
    "v_mfma_f32_16x16x32_bf16 v[84:87], v[32:35], a[32:35], v[84:87]\n"
    "v_mfma_f32_16x16x32_bf16 v[44:47], a[24:27], v[32:35], v[44:47]\n"
    "s_mul_i32 s10, s78, 0x18000\n"
    "ds_read_b128 v[32:35], v4 offset:20480\n"
    "v_mfma_f32_16x16x32_bf16 v[68:71], a[28:31], v[36:39], v[68:71]\n"
    "ds_read_b64_tr_b16 a[72:73], v9 offset:16384\n"
    "v_mfma_f32_16x16x32_bf16 v[88:91], v[36:39], a[32:35], v[88:91]\n"
    "ds_read_b64_tr_b16 a[74:75], v9 offset:16896\n"
    "v_mfma_f32_16x16x32_bf16 v[48:51], a[24:27], v[36:39], v[48:51]\n"
    "ds_read_b64_tr_b16 a[76:77], v9 offset:17408\n"
    "ds_read_b128 v[36:39], v4 offset:24576\n"
    "v_mfma_f32_16x16x32_bf16 v[72:75], a[28:31], v[128:131], v[72:75]\n"
    "ds_read_b64_tr_b16 a[78:79], v9 offset:17920\n"
    "buffer_load_dwordx4 v11, s[20:23], s10 offen lds\n"
    "v_mfma_f32_16x16x32_bf16 v[92:95], v[128:131], a[32:35], v[92:95]\n"
    "v_mfma_f32_16x16x32_bf16 v[52:55], a[24:27], v[128:131], v[52:55]\n"
    "ds_read_b128 v[128:131], v4 offset:28672\n"
    "s_waitcnt lgkmcnt(9)\n"
    "v_mfma_f32_16x16x32_bf16 v[56:59], a[40:43], v[132:135], v[56:59]\n"
    "ds_read_b64_tr_b16 a[80:81], v9 offset:18432\n"
    "v_mfma_f32_16x16x32_bf16 v[76:79], v[132:135], a[44:47], v[76:79]\n"
    "ds_read_b64_tr_b16 a[82:83], v9 offset:18944\n"
    "ds_read_b128 v[132:135], v4 offset:32768\n"
    "v_mfma_f32_16x16x32_bf16 v[60:63], a[40:43], v[16:19], v[60:63]\n"
    "ds_read_b64_tr_b16 a[84:85], v9 offset:28672\n"
    "buffer_load_dwordx4 v11, s[20:23], s10 offen offset:1024 lds\n"
    "v_mfma_f32_16x16x32_bf16 v[80:83], v[16:19], a[44:47], v[80:83]\n"
    "ds_read_b64_tr_b16 a[86:87], v9 offset:29184\n"
    "v_mfma_f32_16x16x32_bf16 v[40:43], a[36:39], v[16:19], v[40:43]\n"
    "ds_read_b128 v[16:19], v4 offset:36864\n"
    "v_mfma_f32_16x16x32_bf16 v[64:67], a[40:43], v[20:23], v[64:67]\n"
    "ds_read_b64_tr_b16 a[88:89], v9 offset:29696\n"
    "v_mfma_f32_16x16x32_bf16 v[84:87], v[20:23], a[44:47], v[84:87]\n"
    "ds_read_b64_tr_b16 a[90:91], v9 offset:30208\n"
    "v_mfma_f32_16x16x32_bf16 v[44:47], a[36:39], v[20:23], v[44:47]\n"
    "ds_read_b128 v[20:23], v5 offset:21504\n"
    "s_waitcnt lgkmcnt(15)\n"
    "v_mfma_f32_16x16x32_bf16 v[68:71], a[40:43], v[24:27], v[68:71]\n"
    "ds_read_b64_tr_b16 a[92:93], v9 offset:30720\n"
    "buffer_load_dwordx4 v11, s[20:23], s10 offen offset:2048 lds\n"
    "v_mfma_f32_16x16x32_bf16 v[88:91], v[24:27], a[44:47], v[88:91]\n"
    "ds_read_b64_tr_b16 a[94:95], v9 offset:31232\n"
    "s_add_u32 m0, s93, 0x23000\n"
    "v_mfma_f32_16x16x32_bf16 v[48:51], a[36:39], v[24:27], v[48:51]\n"
    "ds_read_b128 v[24:27], v5 offset:25600\n"
    "v_mfma_f32_16x16x32_bf16 v[72:75], a[40:43], v[28:31], v[72:75]\n"
    "s_mul_i32 s10, s79, 0x18000\n"
    "v_mfma_f32_16x16x32_bf16 v[92:95], v[28:31], a[44:47], v[92:95]\n"
    "v_mfma_f32_16x16x32_bf16 v[52:55], a[36:39], v[28:31], v[52:55]\n"
    "buffer_load_dwordx4 v11, s[20:23], s10 offen lds\n"
    "ds_read_b128 v[28:31], v5 offset:29696\n"
    "v_mfma_f32_16x16x32_bf16 v[56:59], a[52:55], v[32:35], v[56:59]\n"
    "v_mfma_f32_16x16x32_bf16 v[76:79], v[32:35], a[56:59], v[76:79]\n"
    "ds_read_b128 v[32:35], v5 offset:33792\n"
    "s_waitcnt lgkmcnt(11)\n"
    "v_mfma_f32_16x16x32_bf16 v[60:63], a[52:55], v[36:39], v[60:63]\n"
    "v_mfma_f32_16x16x32_bf16 v[80:83], v[36:39], a[56:59], v[80:83]\n"
    "buffer_load_dwordx4 v11, s[20:23], s10 offen offset:1024 lds\n"
    "buffer_load_dwordx4 v11, s[20:23], s10 offen offset:2048 lds\n"
    "v_mfma_f32_16x16x32_bf16 v[40:43], a[48:51], v[36:39], v[40:43]\n"
    "s_waitcnt vmcnt(21)\n"
    "s_waitcnt lgkmcnt(3)\n"
    "s_barrier\n"
    "ds_read_b128 v[36:39], v5 offset:37888\n"
    "v_mfma_f32_16x16x32_bf16 v[64:67], a[52:55], v[128:131], v[64:67]\n"
    "s_add_u32 m0, s93, 0x14000\n"
    "v_mfma_f32_16x16x32_bf16 v[84:87], v[128:131], a[56:59], v[84:87]\n"
    "v_mfma_f32_16x16x32_bf16 v[44:47], a[48:51], v[128:131], v[44:47]\n"
    "s_mul_i32 s10, s80, 0x18000\n"
    "ds_read_b128 v[128:131], v4 offset:22528\n"
    "v_mfma_f32_16x16x32_bf16 v[68:71], a[52:55], v[132:135], v[68:71]\n"
    "ds_read_b64_tr_b16 a[96:97], v9 offset:40960\n"
    "v_mfma_f32_16x16x32_bf16 v[88:91], v[132:135], a[56:59], v[88:91]\n"
    "ds_read_b64_tr_b16 a[98:99], v9 offset:41472\n"
    "v_mfma_f32_16x16x32_bf16 v[48:51], a[48:51], v[132:135], v[48:51]\n"
    "ds_read_b128 v[132:135], v4 offset:26624\n"
    "s_waitcnt lgkmcnt(7)\n"
    "v_mfma_f32_16x16x32_bf16 v[72:75], a[52:55], v[16:19], v[72:75]\n"
    "ds_read_b64_tr_b16 a[100:101], v9 offset:41984\n"
    "buffer_load_dwordx4 v11, s[20:23], s10 offen lds\n"
    "v_mfma_f32_16x16x32_bf16 v[92:95], v[16:19], a[56:59], v[92:95]\n"
    "ds_read_b64_tr_b16 a[102:103], v9 offset:42496\n"
    "v_mfma_f32_16x16x32_bf16 v[52:55], a[48:51], v[16:19], v[52:55]\n"
    "ds_read_b128 v[16:19], v4 offset:30720\n"
    "v_mfma_f32_16x16x32_bf16 v[56:59], a[64:67], v[20:23], v[56:59]\n"
    "ds_read_b64_tr_b16 a[104:105], v9 offset:43008\n"
    "v_mfma_f32_16x16x32_bf16 v[76:79], v[20:23], a[68:71], v[76:79]\n"
    "ds_read_b64_tr_b16 a[106:107], v9 offset:43520\n"
    "ds_read_b128 v[20:23], v4 offset:34816\n"
    "v_mfma_f32_16x16x32_bf16 v[60:63], a[64:67], v[24:27], v[60:63]\n"
    "ds_read_b64_tr_b16 a[108:109], v9 offset:53248\n"
    "buffer_load_dwordx4 v11, s[20:23], s10 offen offset:1024 lds\n"
    "v_mfma_f32_16x16x32_bf16 v[80:83], v[24:27], a[68:71], v[80:83]\n"
    "ds_read_b64_tr_b16 a[110:111], v9 offset:53760\n"
    "v_mfma_f32_16x16x32_bf16 v[40:43], a[60:63], v[24:27], v[40:43]\n"
    "ds_read_b128 v[24:27], v4 offset:38912\n"
    "s_waitcnt lgkmcnt(13)\n"
    "v_mfma_f32_16x16x32_bf16 v[64:67], a[64:67], v[28:31], v[64:67]\n"
    "v_mfma_f32_16x16x32_bf16 v[84:87], v[28:31], a[68:71], v[84:87]\n"
    "ds_read_b64_tr_b16 a[112:113], v9 offset:54272\n"
    "v_mfma_f32_16x16x32_bf16 v[44:47], a[60:63], v[28:31], v[44:47]\n"
    "ds_read_b64_tr_b16 a[114:115], v9 offset:54784\n"
    "ds_read_b128 v[28:31], v5 offset:23552\n"
    "v_mfma_f32_16x16x32_bf16 v[68:71], a[64:67], v[32:35], v[68:71]\n"
    "ds_read_b64_tr_b16 a[116:117], v9 offset:55296\n"
    "buffer_load_dwordx4 v11, s[20:23], s10 offen offset:2048 lds\n"
    "v_mfma_f32_16x16x32_bf16 v[88:91], v[32:35], a[68:71], v[88:91]\n"
    "ds_read_b64_tr_b16 a[118:119], v9 offset:55808\n"
    "s_add_u32 m0, s93, 0x17000\n"
    "v_mfma_f32_16x16x32_bf16 v[48:51], a[60:63], v[32:35], v[48:51]\n"
    "ds_read_b128 v[32:35], v5 offset:27648\n"
    "v_mfma_f32_16x16x32_bf16 v[72:75], a[64:67], v[36:39], v[72:75]\n"
    "s_mul_i32 s10, s81, 0x18000\n"
    "v_mfma_f32_16x16x32_bf16 v[92:95], v[36:39], a[68:71], v[92:95]\n"
    "v_mfma_f32_16x16x32_bf16 v[52:55], a[60:63], v[36:39], v[52:55]\n"
    "ds_read_b128 v[36:39], v5 offset:31744\n"
    "s_waitcnt lgkmcnt(13)\n"
    "v_mfma_f32_16x16x32_bf16 v[56:59], a[76:79], v[128:131], v[56:59]\n"
    "buffer_load_dwordx4 v11, s[20:23], s10 offen lds\n"
    "v_mfma_f32_16x16x32_bf16 v[76:79], v[128:131], a[80:83], v[76:79]\n"
    "ds_read_b128 v[128:131], v5 offset:35840\n"
    "v_mfma_f32_16x16x32_bf16 v[60:63], a[76:79], v[132:135], v[60:63]\n"
    "v_mfma_f32_16x16x32_bf16 v[80:83], v[132:135], a[80:83], v[80:83]\n"
    "buffer_load_dwordx4 v11, s[20:23], s10 offen offset:1024 lds\n"
    "buffer_load_dwordx4 v11, s[20:23], s10 offen offset:2048 lds\n"
    "v_mfma_f32_16x16x32_bf16 v[40:43], a[72:75], v[132:135], v[40:43]\n"
    "s_waitcnt vmcnt(6)\n"
    "s_waitcnt lgkmcnt(3)\n"
    "s_barrier\n"
    "ds_read_b128 v[132:135], v5 offset:39936\n"
    "v_mfma_f32_16x16x32_bf16 v[64:67], a[76:79], v[16:19], v[64:67]\n"
    "s_add_u32 m0, s93, 0x1a000\n"
    "v_mfma_f32_16x16x32_bf16 v[84:87], v[16:19], a[80:83], v[84:87]\n"
    "v_mfma_f32_16x16x32_bf16 v[44:47], a[72:75], v[16:19], v[44:47]\n"
    "ds_read_b128 v[16:19], v4 offset:40960\n"
    "v_mfma_f32_16x16x32_bf16 v[68:71], a[76:79], v[20:23], v[68:71]\n"
    "ds_read_b64_tr_b16 a[120:121], v10\n"
    "s_mul_i32 s10, s82, 0x18000\n"
    "v_mfma_f32_16x16x32_bf16 v[88:91], v[20:23], a[80:83], v[88:91]\n"
    "ds_read_b64_tr_b16 a[122:123], v10 offset:512\n"
    "v_mfma_f32_16x16x32_bf16 v[48:51], a[72:75], v[20:23], v[48:51]\n"
    "ds_read_b128 v[20:23], v4 offset:45056\n"
    "v_mfma_f32_16x16x32_bf16 v[72:75], a[76:79], v[24:27], v[72:75]\n"
    "ds_read_b64_tr_b16 a[124:125], v10 offset:1024\n"
    "buffer_load_dwordx4 v11, s[20:23], s10 offen lds\n"
    "v_mfma_f32_16x16x32_bf16 v[92:95], v[24:27], a[80:83], v[92:95]\n"
    "ds_read_b64_tr_b16 a[126:127], v10 offset:1536\n"
    "v_mfma_f32_16x16x32_bf16 v[52:55], a[72:75], v[24:27], v[52:55]\n"
    "ds_read_b64_tr_b16 a[128:129], v10 offset:2048\n"
    "ds_read_b128 v[24:27], v4 offset:49152\n"
    "v_mfma_f32_16x16x32_bf16 v[56:59], a[88:91], v[28:31], v[56:59]\n"
    "ds_read_b64_tr_b16 a[130:131], v10 offset:2560\n"
    "v_mfma_f32_16x16x32_bf16 v[76:79], v[28:31], a[92:95], v[76:79]\n"
    "ds_read_b128 v[28:31], v4 offset:53248\n"
    "s_waitcnt lgkmcnt(11)\n"
    "v_mfma_f32_16x16x32_bf16 v[60:63], a[88:91], v[32:35], v[60:63]\n"
    "buffer_load_dwordx4 v11, s[20:23], s10 offen offset:1024 lds\n"
    "v_and_b32 v156, 15, v0\n"
    "v_mfma_f32_16x16x32_bf16 v[80:83], v[32:35], a[92:95], v[80:83]\n"
    "ds_read_b64_tr_b16 a[132:133], v10 offset:12288\n"
    "v_and_b32 v157, 1, v0\n"
    "v_mfma_f32_16x16x32_bf16 v[40:43], a[84:87], v[32:35], v[40:43]\n"
    "ds_read_b64_tr_b16 a[134:135], v10 offset:12800\n"
    "ds_read_b128 v[32:35], v4 offset:57344\n"
    "v_mfma_f32_16x16x32_bf16 v[64:67], a[88:91], v[36:39], v[64:67]\n"
    "ds_read_b64_tr_b16 a[136:137], v10 offset:13312\n"
    "v_cmp_eq_u32 s[12:13], 1, v157\n"
    "v_mfma_f32_16x16x32_bf16 v[84:87], v[36:39], a[92:95], v[84:87]\n"
    "ds_read_b64_tr_b16 a[138:139], v10 offset:13824\n"
    "v_lshl_add_u32 v158, v14, 2, v157\n"
    "v_mfma_f32_16x16x32_bf16 v[44:47], a[84:87], v[36:39], v[44:47]\n"
    "buffer_load_dwordx4 v11, s[20:23], s10 offen offset:2048 lds\n"
    "ds_read_b128 v[36:39], v5 offset:41984\n"
    "v_mfma_f32_16x16x32_bf16 v[68:71], a[88:91], v[128:131], v[68:71]\n"
    "ds_read_b64_tr_b16 a[140:141], v10 offset:14336\n"
    "s_add_u32 m0, s93, 0x1d000\n"
    "v_cmp_le_u32 s[14:15], v158, v156\n"
    "v_mfma_f32_16x16x32_bf16 v[88:91], v[128:131], a[92:95], v[88:91]\n"
    "ds_read_b64_tr_b16 a[142:143], v10 offset:14848\n"
    "v_mfma_f32_16x16x32_bf16 v[48:51], a[84:87], v[128:131], v[48:51]\n"
    "v_add_u32 v158, 2, v158\n"
    "ds_read_b128 v[128:131], v5 offset:46080\n"
    "s_waitcnt lgkmcnt(15)\n"
    "v_mfma_f32_16x16x32_bf16 v[72:75], a[88:91], v[132:135], v[72:75]\n"
    "s_mul_i32 s10, s83, 0x18000\n"
    "v_cmp_le_u32 s[100:101], v158, v156\n"
    "v_mfma_f32_16x16x32_bf16 v[92:95], v[132:135], a[92:95], v[92:95]\n"
    "v_lshlrev_b32 v123, 4, v156\n"
    "v_mfma_f32_16x16x32_bf16 v[52:55], a[84:87], v[132:135], v[52:55]\n"
    "buffer_load_dwordx4 v11, s[20:23], s10 offen lds\n"
    "ds_read_b128 v[132:135], v5 offset:50176\n"
    "v_mfma_f32_16x16x32_bf16 v[56:59], a[100:103], v[16:19], v[56:59]\n"
    "v_mov_b32 v156, 0xc0c0100\n"
    "v_mfma_f32_16x16x32_bf16 v[76:79], v[16:19], a[104:107], v[76:79]\n"
    "v_mov_b32 v157, 0x1000c0c\n"
    "ds_read_b128 v[16:19], v5 offset:54272\n"
    "v_mfma_f32_16x16x32_bf16 v[60:63], a[100:103], v[20:23], v[60:63]\n"
    "v_cndmask_b32 v156, v156, v157, s[12:13]\n"
    "v_mfma_f32_16x16x32_bf16 v[80:83], v[20:23], a[104:107], v[80:83]\n"
    "buffer_load_dwordx4 v11, s[20:23], s10 offen offset:1024 lds\n"
    "buffer_load_dwordx4 v11, s[20:23], s10 offen offset:2048 lds\n"
    "v_mov_b32 v158, 0xc0c0c0c\n"
    "v_mfma_f32_16x16x32_bf16 v[40:43], a[96:99], v[20:23], v[40:43]\n"
    "s_waitcnt vmcnt(6)\n"
    "s_waitcnt lgkmcnt(3)\n"
    "s_barrier\n"
    "ds_read_b128 v[20:23], v5 offset:58368\n"
    "v_mfma_f32_16x16x32_bf16 v[64:67], a[100:103], v[24:27], v[64:67]\n"
    "s_add_u32 s7, s86, 0\n"
    "v_cndmask_b32 v124, v158, v156, s[14:15]\n"
    "v_mfma_f32_16x16x32_bf16 v[84:87], v[24:27], a[104:107], v[84:87]\n"
    "s_and_b32 s7, s7, 3\n"
    "s_lshl_b32 s38, s62, 3\n"
    "v_mfma_f32_16x16x32_bf16 v[44:47], a[96:99], v[24:27], v[44:47]\n"
    "s_lshl_b32 s7, s7, 15\n"
    "v_cndmask_b32 v125, v156, v158, s[14:15]\n"
    "ds_read_b128 v[24:27], v4 offset:43008\n"
    "v_mfma_f32_16x16x32_bf16 v[68:71], a[100:103], v[28:31], v[68:71]\n"
    "ds_read_b64_tr_b16 a[144:145], v9 offset:16384\n"
    "v_mfma_f32_16x16x32_bf16 v[88:91], v[28:31], a[104:107], v[88:91]\n"
    "ds_read_b64_tr_b16 a[146:147], v9 offset:16896\n"
    "s_add_u32 m0, s97, 0\n"
    "v_mov_b32 v157, 0xc0c0302\n"
    "v_mfma_f32_16x16x32_bf16 v[48:51], a[96:99], v[28:31], v[48:51]\n"
    "ds_read_b64_tr_b16 a[148:149], v9 offset:17408\n"
    "ds_read_b128 v[28:31], v4 offset:47104\n"
    "v_mfma_f32_16x16x32_bf16 v[72:75], a[100:103], v[32:35], v[72:75]\n"
    "ds_read_b64_tr_b16 a[150:151], v9 offset:17920\n"
    "s_add_u32 s38, s38, s63\n"
    "v_mov_b32 v159, 0x3020c0c\n"
    "v_mfma_f32_16x16x32_bf16 v[92:95], v[32:35], a[104:107], v[92:95]\n"
    "v_add_u32 v137, s7, v136\n"
    "v_mfma_f32_16x16x32_bf16 v[52:55], a[96:99], v[32:35], v[52:55]\n"
    "v_cndmask_b32 v157, v157, v159, s[12:13]\n"
    "ds_read_b128 v[32:35], v4 offset:51200\n"
    "s_waitcnt lgkmcnt(9)\n"
    "v_mfma_f32_16x16x32_bf16 v[56:59], a[112:115], v[36:39], v[56:59]\n"
    "ds_read_b64_tr_b16 a[152:153], v9 offset:18432\n"
    "buffer_load_dwordx4 v137, s[24:27], 0 offen lds\n"
    "v_mfma_f32_16x16x32_bf16 v[76:79], v[36:39], a[116:119], v[76:79]\n"
    "ds_read_b64_tr_b16 a[154:155], v9 offset:18944\n"
    "v_cndmask_b32 v126, v158, v157, s[100:101]\n"
    "ds_read_b128 v[36:39], v4 offset:55296\n"
    "v_mfma_f32_16x16x32_bf16 v[60:63], a[112:115], v[128:131], v[60:63]\n"
    "ds_read_b64_tr_b16 a[156:157], v9 offset:28672\n"
    "s_lshl_b32 s38, s38, 2\n"
    "v_mfma_f32_16x16x32_bf16 v[80:83], v[128:131], a[116:119], v[80:83]\n"
    "ds_read_b64_tr_b16 a[158:159], v9 offset:29184\n"
    "v_cndmask_b32 v127, v157, v158, s[100:101]\n"
    "v_mfma_f32_16x16x32_bf16 v[40:43], a[108:111], v[128:131], v[40:43]\n"
    "s_add_u32 s38, s38, s6\n"
    "ds_read_b128 v[128:131], v4 offset:59392\n"
    "v_mfma_f32_16x16x32_bf16 v[64:67], a[112:115], v[132:135], v[64:67]\n"
    "ds_read_b64_tr_b16 a[160:161], v9 offset:29696\n"
    "buffer_load_dwordx4 v137, s[24:27], s89 offen offset:1088 lds\n"
    "s_mov_b32 s59, 0x7f800000\n"
    "v_mfma_f32_16x16x32_bf16 v[84:87], v[132:135], a[116:119], v[84:87]\n"
    "ds_read_b64_tr_b16 a[162:163], v9 offset:30208\n"
    "v_mfma_f32_16x16x32_bf16 v[44:47], a[108:111], v[132:135], v[44:47]\n"
    "v_lshrrev_b32 v157, 1, v14\n"
    "ds_read_b128 v[132:135], v5 offset:44032\n"
    "s_waitcnt lgkmcnt(15)\n"
    "v_mfma_f32_16x16x32_bf16 v[68:71], a[112:115], v[16:19], v[68:71]\n"
    "ds_read_b64_tr_b16 a[164:165], v9 offset:30720\n"
    "v_lshl_add_u32 v123, v157, 8, v123\n"
    "v_mfma_f32_16x16x32_bf16 v[88:91], v[16:19], a[116:119], v[88:91]\n"
    "ds_read_b64_tr_b16 a[166:167], v9 offset:31232\n"
    "s_mul_i32 s38, s38, 0x1600\n"
    "v_mfma_f32_16x16x32_bf16 v[48:51], a[108:111], v[16:19], v[48:51]\n"
    "buffer_load_dwordx4 v137, s[24:27], s90 offen offset:2176 lds\n"
    "v_and_b32 v157, 1, v14\n"
    "ds_read_b128 v[16:19], v5 offset:48128\n"
    "v_mfma_f32_16x16x32_bf16 v[72:75], a[112:115], v[20:23], v[72:75]\n"
    "s_add_u32 s38, s38, 0x600000\n"
    "v_mfma_f32_16x16x32_bf16 v[92:95], v[20:23], a[116:119], v[92:95]\n"
    "s_add_u32 s39, s38, 0x1000\n"
    "v_lshl_add_u32 v123, v157, 14, v123\n"
    "v_mfma_f32_16x16x32_bf16 v[52:55], a[108:111], v[20:23], v[52:55]\n"
    "s_and_b32 s56, s6, 1\n"
    "ds_read_b128 v[20:23], v5 offset:52224\n"
    "v_mfma_f32_16x16x32_bf16 v[56:59], a[124:127], v[24:27], v[56:59]\n"
    "s_mov_b32 s71, 0x2000000\n"
    "s_lshl_b32 s56, s56, 9\n"
    "v_mfma_f32_16x16x32_bf16 v[76:79], v[24:27], a[128:131], v[76:79]\n"
    "ds_read_b128 v[24:27], v5 offset:56320\n"
    "s_waitcnt lgkmcnt(11)\n"
    "v_mfma_f32_16x16x32_bf16 v[60:63], a[124:127], v[28:31], v[60:63]\n"
    "buffer_load_dwordx4 v137, s[24:27], s91 offen offset:3264 lds\n"
    "s_lshl_b32 s11, s2, 2\n"
    "s_lshl_b32 s57, s64, 16\n"
    "v_mfma_f32_16x16x32_bf16 v[80:83], v[28:31], a[128:131], v[80:83]\n"
    "s_add_u32 s11, s11, s6\n"
    "s_mul_i32 s11, s11, 11\n"
    "v_min_u32 v153, 10, v0\n"
    "v_add_u32 v153, s11, v153\n"
    "v_lshlrev_b32 v153, 7, v153\n"
    "buffer_load_dword v152, v153, s[32:35], 0 offen\n"
    "v_mfma_f32_16x16x32_bf16 v[40:43], a[120:123], v[28:31], v[40:43]\n"
    "s_waitcnt vmcnt(5)\n"
    "s_waitcnt lgkmcnt(3)\n"
    "s_barrier\n"
    "ds_read_b128 v[28:31], v5 offset:60416\n"
    "v_mfma_f32_16x16x32_bf16 v[64:67], a[124:127], v[32:35], v[64:67]\n"
    "s_add_u32 s7, s86, 1\n"
    "v_mfma_f32_16x16x32_bf16 v[84:87], v[32:35], a[128:131], v[84:87]\n"
    "s_add_u32 s56, s56, s57\n"
    "s_lshl_b32 s57, s65, 11\n"
    "v_mfma_f32_16x16x32_bf16 v[44:47], a[120:123], v[32:35], v[44:47]\n"
    "s_and_b32 s7, s7, 3\n"
    "ds_read_b128 v[32:35], v6\n"
    "v_mfma_f32_16x16x32_bf16 v[68:71], a[124:127], v[36:39], v[68:71]\n"
    "ds_read_b64_tr_b16 a[168:169], v9 offset:40960\n"
    "s_add_u32 s56, s56, s57\n"
    "v_mfma_f32_16x16x32_bf16 v[88:91], v[36:39], a[128:131], v[88:91]\n"
    "ds_read_b64_tr_b16 a[170:171], v9 offset:41472\n"
    "s_lshr_b32 s57, s6, 1\n"
    "v_mfma_f32_16x16x32_bf16 v[48:51], a[120:123], v[36:39], v[48:51]\n"
    "ds_read_b128 v[36:39], v6 offset:4096\n"
    "s_waitcnt lgkmcnt(7)\n"
    "v_mfma_f32_16x16x32_bf16 v[72:75], a[124:127], v[128:131], v[72:75]\n"
    "ds_read_b64_tr_b16 a[172:173], v9 offset:41984\n"
    "s_lshl_b32 s57, s57, 10\n"
    "v_mfma_f32_16x16x32_bf16 v[92:95], v[128:131], a[128:131], v[92:95]\n"
    "ds_read_b64_tr_b16 a[174:175], v9 offset:42496\n"
    "s_lshl_b32 s7, s7, 15\n"
    "v_mfma_f32_16x16x32_bf16 v[52:55], a[120:123], v[128:131], v[52:55]\n"
    "s_add_u32 s56, s56, s57\n"
    "ds_read_b128 v[128:131], v6 offset:8192\n"
    "v_mfma_f32_16x16x32_bf16 v[56:59], a[136:139], v[132:135], v[56:59]\n"
    "ds_read_b64_tr_b16 a[176:177], v9 offset:43008\n"
    "s_add_u32 s88, s56, 0x100000\n"
    "v_mfma_f32_16x16x32_bf16 v[76:79], v[132:135], a[140:143], v[76:79]\n"
    "ds_read_b64_tr_b16 a[178:179], v9 offset:43520\n"
    "ds_read_b128 v[132:135], v6 offset:12288\n"
    "v_mfma_f32_16x16x32_bf16 v[60:63], a[136:139], v[16:19], v[60:63]\n"
    "ds_read_b64_tr_b16 a[180:181], v9 offset:53248\n"
    "s_add_u32 m0, s97, 0x5000\n"
    "v_mfma_f32_16x16x32_bf16 v[80:83], v[16:19], a[140:143], v[80:83]\n"
    "ds_read_b64_tr_b16 a[182:183], v9 offset:53760\n"
    "v_mfma_f32_16x16x32_bf16 v[40:43], a[132:135], v[16:19], v[40:43]\n"
    "ds_read_b128 v[16:19], v6 offset:16384\n"
    "s_waitcnt lgkmcnt(13)\n"
    "v_mfma_f32_16x16x32_bf16 v[64:67], a[136:139], v[20:23], v[64:67]\n"
    "v_add_u32 v137, s7, v136\n"
    "v_mfma_f32_16x16x32_bf16 v[84:87], v[20:23], a[140:143], v[84:87]\n"
    "ds_read_b64_tr_b16 a[184:185], v9 offset:54272\n"
    "v_mfma_f32_16x16x32_bf16 v[44:47], a[132:135], v[20:23], v[44:47]\n"
    "ds_read_b64_tr_b16 a[186:187], v9 offset:54784\n"
    "ds_read_b128 v[20:23], v7 offset:1024\n"
    "v_mfma_f32_16x16x32_bf16 v[68:71], a[136:139], v[24:27], v[68:71]\n"
    "ds_read_b64_tr_b16 a[188:189], v9 offset:55296\n"
    "v_mfma_f32_16x16x32_bf16 v[88:91], v[24:27], a[140:143], v[88:91]\n"
    "ds_read_b64_tr_b16 a[190:191], v9 offset:55808\n"
    "v_mfma_f32_16x16x32_bf16 v[48:51], a[132:135], v[24:27], v[48:51]\n"
    "buffer_load_dwordx4 v137, s[24:27], 0 offen lds\n"
    "ds_read_b128 v[24:27], v7 offset:5120\n"
    "v_mfma_f32_16x16x32_bf16 v[72:75], a[136:139], v[28:31], v[72:75]\n"
    "s_lshl_b32 s93, s6, 11\n"
    "v_mfma_f32_16x16x32_bf16 v[92:95], v[28:31], a[140:143], v[92:95]\n"
    "v_mfma_f32_16x16x32_bf16 v[52:55], a[132:135], v[28:31], v[52:55]\n"
    "ds_read_b128 v[28:31], v7 offset:9216\n"
    "s_waitcnt lgkmcnt(13)\n"
    "v_mfma_f32_16x16x32_bf16 v[56:59], a[148:151], v[32:35], v[56:59]\n"
    "v_mfma_f32_16x16x32_bf16 v[76:79], v[32:35], a[152:155], v[76:79]\n"
    "buffer_load_dwordx4 v137, s[24:27], s89 offen offset:1088 lds\n"
    "ds_read_b128 v[32:35], v7 offset:13312\n"
    "v_mfma_f32_16x16x32_bf16 v[60:63], a[148:151], v[36:39], v[60:63]\n"
    "v_mfma_f32_16x16x32_bf16 v[80:83], v[36:39], a[152:155], v[80:83]\n"
    "buffer_load_dwordx4 v137, s[24:27], s90 offen offset:2176 lds\n"
    "buffer_load_dwordx4 v137, s[24:27], s91 offen offset:3264 lds\n"
    "v_mfma_f32_16x16x32_bf16 v[40:43], a[144:147], v[36:39], v[40:43]\n"
    "s_waitcnt lgkmcnt(3)\n"
    "s_barrier\n"
    "ds_read_b128 v[36:39], v7 offset:17408\n"
    "v_mfma_f32_16x16x32_bf16 v[64:67], a[148:151], v[128:131], v[64:67]\n"
    "v_mfma_f32_16x16x32_bf16 v[84:87], v[128:131], a[152:155], v[84:87]\n"
    "s_add_u32 s7, s86, 3\n"
    "v_mfma_f32_16x16x32_bf16 v[44:47], a[144:147], v[128:131], v[44:47]\n"
    "ds_read_b128 v[128:131], v6 offset:2048\n"
    "v_mfma_f32_16x16x32_bf16 v[68:71], a[148:151], v[132:135], v[68:71]\n"
    "v_mfma_f32_16x16x32_bf16 v[88:91], v[132:135], a[152:155], v[88:91]\n"
    "s_and_b32 s7, s7, 3\n"
    "v_mfma_f32_16x16x32_bf16 v[48:51], a[144:147], v[132:135], v[48:51]\n"
    "ds_read_b128 v[132:135], v6 offset:6144\n"
    "v_mfma_f32_16x16x32_bf16 v[72:75], a[148:151], v[16:19], v[72:75]\n"
    "v_mfma_f32_16x16x32_bf16 v[92:95], v[16:19], a[152:155], v[92:95]\n"
    "s_lshl_b32 s7, s7, 15\n"
    "v_mfma_f32_16x16x32_bf16 v[52:55], a[144:147], v[16:19], v[52:55]\n"
    "ds_read_b128 v[16:19], v6 offset:10240\n"
    "v_mfma_f32_16x16x32_bf16 v[56:59], a[160:163], v[20:23], v[56:59]\n"
    "v_mfma_f32_16x16x32_bf16 v[76:79], v[20:23], a[164:167], v[76:79]\n"
    "ds_read_b128 v[20:23], v6 offset:14336\n"
    "s_waitcnt lgkmcnt(5)\n"
    "v_mfma_f32_16x16x32_bf16 v[60:63], a[160:163], v[24:27], v[60:63]\n"
    "s_add_u32 m0, s97, 0x20000\n"
    "v_mfma_f32_16x16x32_bf16 v[80:83], v[24:27], a[164:167], v[80:83]\n"
    "v_mfma_f32_16x16x32_bf16 v[40:43], a[156:159], v[24:27], v[40:43]\n"
    "v_add_u32 v137, s7, v136\n"
    "ds_read_b128 v[24:27], v6 offset:18432\n"
    "v_mfma_f32_16x16x32_bf16 v[64:67], a[160:163], v[28:31], v[64:67]\n"
    "v_mfma_f32_16x16x32_bf16 v[84:87], v[28:31], a[164:167], v[84:87]\n"
    "v_mfma_f32_16x16x32_bf16 v[44:47], a[156:159], v[28:31], v[44:47]\n"
    "buffer_load_dwordx4 v137, s[24:27], 0 offen lds\n"
    "ds_read_b128 v[28:31], v7 offset:3072\n"
    "v_mfma_f32_16x16x32_bf16 v[68:71], a[160:163], v[32:35], v[68:71]\n"
    "v_mfma_f32_16x16x32_bf16 v[88:91], v[32:35], a[164:167], v[88:91]\n"
    "v_mov_b32 v12, v112\n"
    "v_mfma_f32_16x16x32_bf16 v[48:51], a[156:159], v[32:35], v[48:51]\n"
    "ds_read_b128 v[32:35], v7 offset:7168\n"
    "s_waitcnt lgkmcnt(5)\n"
    "v_mfma_f32_16x16x32_bf16 v[72:75], a[160:163], v[36:39], v[72:75]\n"
    "v_mfma_f32_16x16x32_bf16 v[92:95], v[36:39], a[164:167], v[92:95]\n"
    "buffer_load_dwordx4 v137, s[24:27], s89 offen offset:1088 lds\n"
    "v_mfma_f32_16x16x32_bf16 v[52:55], a[156:159], v[36:39], v[52:55]\n"
    "ds_read_b128 v[36:39], v7 offset:11264\n"
    "v_mfma_f32_16x16x32_bf16 v[56:59], a[172:175], v[128:131], v[56:59]\n"
    "v_mfma_f32_16x16x32_bf16 v[76:79], v[128:131], a[176:179], v[76:79]\n"
    "ds_read_b128 v[128:131], v7 offset:15360\n"
    "v_mfma_f32_16x16x32_bf16 v[60:63], a[172:175], v[132:135], v[60:63]\n"
    "v_mfma_f32_16x16x32_bf16 v[80:83], v[132:135], a[176:179], v[80:83]\n"
    "buffer_load_dwordx4 v137, s[24:27], s90 offen offset:2176 lds\n"
    "v_mfma_f32_16x16x32_bf16 v[40:43], a[168:171], v[132:135], v[40:43]\n"
    "ds_read_b128 v[132:135], v7 offset:19456\n"
    "s_waitcnt lgkmcnt(5)\n"
    "v_mfma_f32_16x16x32_bf16 v[64:67], a[172:175], v[16:19], v[64:67]\n"
    "v_mfma_f32_16x16x32_bf16 v[84:87], v[16:19], a[176:179], v[84:87]\n"
    "v_mfma_f32_16x16x32_bf16 v[44:47], a[168:171], v[16:19], v[44:47]\n"
    "v_mfma_f32_16x16x32_bf16 v[68:71], a[172:175], v[20:23], v[68:71]\n"
    "buffer_load_dwordx4 v137, s[24:27], s91 offen offset:3264 lds\n"
    "v_mfma_f32_16x16x32_bf16 v[88:91], v[20:23], a[176:179], v[88:91]\n"
    "s_add_u32 s7, s86, 2\n"
    "v_mfma_f32_16x16x32_bf16 v[48:51], a[168:171], v[20:23], v[48:51]\n"
    "v_mfma_f32_16x16x32_bf16 v[72:75], a[172:175], v[24:27], v[72:75]\n"
    "s_and_b32 s7, s7, 3\n"
    "v_mfma_f32_16x16x32_bf16 v[92:95], v[24:27], a[176:179], v[92:95]\n"
    "v_mfma_f32_16x16x32_bf16 v[52:55], a[168:171], v[24:27], v[52:55]\n"
    "s_waitcnt lgkmcnt(2)\n"
    "v_mfma_f32_16x16x32_bf16 v[56:59], a[184:187], v[28:31], v[56:59]\n"
    "s_lshl_b32 s7, s7, 15\n"
    "v_mfma_f32_16x16x32_bf16 v[76:79], v[28:31], a[188:191], v[76:79]\n"
    "v_mfma_f32_16x16x32_bf16 v[60:63], a[184:187], v[32:35], v[60:63]\n"
    "s_add_u32 m0, s97, 0xa000\n"
    "v_mfma_f32_16x16x32_bf16 v[80:83], v[32:35], a[188:191], v[80:83]\n"
    "v_mov_b32 v11, v113\n"
    "v_mfma_f32_16x16x32_bf16 v[40:43], a[180:183], v[32:35], v[40:43]\n"
    "v_add_u32 v137, s7, v136\n"
    "v_mfma_f32_16x16x32_bf16 v[64:67], a[184:187], v[36:39], v[64:67]\n"
    "v_mfma_f32_16x16x32_bf16 v[84:87], v[36:39], a[188:191], v[84:87]\n"
    "v_mfma_f32_16x16x32_bf16 v[44:47], a[180:183], v[36:39], v[44:47]\n"
    "buffer_load_dwordx4 v137, s[24:27], 0 offen lds\n"
    "s_waitcnt lgkmcnt(0)\n"
    "v_mfma_f32_16x16x32_bf16 v[68:71], a[184:187], v[128:131], v[68:71]\n"
    "v_mfma_f32_16x16x32_bf16 v[88:91], v[128:131], a[188:191], v[88:91]\n"
    "v_mfma_f32_16x16x32_bf16 v[48:51], a[180:183], v[128:131], v[48:51]\n"
    "v_mfma_f32_16x16x32_bf16 v[72:75], a[184:187], v[132:135], v[72:75]\n"
    "v_mfma_f32_16x16x32_bf16 v[92:95], v[132:135], a[188:191], v[92:95]\n"
    "buffer_load_dwordx4 v137, s[24:27], s89 offen offset:1088 lds\n"
    "buffer_load_dwordx4 v137, s[24:27], s90 offen offset:2176 lds\n"
    "buffer_load_dwordx4 v137, s[24:27], s91 offen offset:3264 lds\n"
    "v_mfma_f32_16x16x32_bf16 v[52:55], a[180:183], v[132:135], v[52:55]\n"
    "s_barrier\n"
    "s_cmp_eq_u32 s64, 0\n"
    "s_cselect_b32 s58, 0xcf6e6b28, s59\n"
    "v_mov_b32 v19, v143\n"
    "v_cvt_pk_bf16_f32 v104, v56, v57\n"
    "v_cvt_pk_bf16_f32 v105, v58, v59\n"
    "v_cvt_pk_bf16_f32 v96, v40, v41\n"
    "v_cvt_pk_bf16_f32 v97, v42, v43\n"
    "v_cvt_pk_bf16_f32 v106, v60, v61\n"
    "v_cvt_pk_bf16_f32 v107, v62, v63\n"
    "v_cvt_pk_bf16_f32 v98, v44, v45\n"
    "v_cvt_pk_bf16_f32 v99, v46, v47\n"
    "v_cvt_pk_bf16_f32 v108, v64, v65\n"
    "v_cvt_pk_bf16_f32 v109, v66, v67\n"
    "s_add_u32 m0, s93, 0xf000\n"
    "s_mov_b32 s10, 0\n"
    "buffer_load_dwordx4 v12, s[28:31], s10 offen lds\n"
    "v_cvt_pk_bf16_f32 v100, v48, v49\n"
    "v_cvt_pk_bf16_f32 v101, v50, v51\n"
    "v_cvt_pk_bf16_f32 v110, v68, v69\n"
    "v_cvt_pk_bf16_f32 v111, v70, v71\n"
    "v_cvt_pk_bf16_f32 v102, v52, v53\n"
    "v_cvt_pk_bf16_f32 v103, v54, v55\n"
    "v_cvt_pk_bf16_f32 v112, v72, v73\n"
    "v_cvt_pk_bf16_f32 v113, v74, v75\n"
    "v_mfma_f32_16x16x16_bf16 v[40:43], v[104:105], v[96:97], 0\n"
    "v_cvt_pk_bf16_f32 v160, v76, v77\n"
    "v_mfma_f32_16x16x16_bf16 v[44:47], v[106:107], v[96:97], 0\n"
    "v_cvt_pk_bf16_f32 v161, v78, v79\n"
    "v_mfma_f32_16x16x16_bf16 v[48:51], v[106:107], v[98:99], 0\n"
    "v_cvt_pk_bf16_f32 v162, v80, v81\n"
    "v_mfma_f32_16x16x16_bf16 v[52:55], v[108:109], v[98:99], 0\n"
    "v_cvt_pk_bf16_f32 v163, v82, v83\n"
    "v_cvt_pk_bf16_f32 v164, v84, v85\n"
    "v_mfma_f32_16x16x16_bf16 v[56:59], v[108:109], v[100:101], 0\n"
    "v_cvt_pk_bf16_f32 v165, v86, v87\n"
    "v_mfma_f32_16x16x16_bf16 v[60:63], v[110:111], v[100:101], 0\n"
    "v_cvt_pk_bf16_f32 v166, v88, v89\n"
    "v_mfma_f32_16x16x16_bf16 v[64:67], v[110:111], v[102:103], 0\n"
    "v_cvt_pk_bf16_f32 v167, v90, v91\n"
    "v_mfma_f32_16x16x16_bf16 v[68:71], v[112:113], v[102:103], 0\n"
    "v_cvt_pk_bf16_f32 v168, v92, v93\n"
    "buffer_load_dwordx4 v11, s[28:31], s10 offen offset:1024 lds\n"
    "v_cvt_pk_bf16_f32 v169, v94, v95\n"
    "v_cndmask_b32 v40, v40, v41, s[12:13]\n"
    "v_min_f32 v40, s58, v40\n"
    "v_cndmask_b32 v42, v42, v43, s[12:13]\n"
    "v_min_f32 v42, s58, v42\n"
    "v_cndmask_b32 v44, v44, v45, s[12:13]\n"
    "v_cndmask_b32 v46, v46, v47, s[12:13]\n"
    "v_cndmask_b32 v44, v40, v44, s[14:15]\n"
    "v_cndmask_b32 v46, v42, v46, s[100:101]\n"
    "v_cndmask_b32 v48, v48, v49, s[12:13]\n"
    "v_cndmask_b32 v50, v50, v51, s[12:13]\n"
    "v_cndmask_b32 v52, v52, v53, s[12:13]\n"
    "v_cndmask_b32 v54, v54, v55, s[12:13]\n"
    "v_cndmask_b32 v52, v48, v52, s[14:15]\n"
    "v_cndmask_b32 v54, v50, v54, s[100:101]\n"
    "v_cndmask_b32 v56, v56, v57, s[12:13]\n"
    "v_cndmask_b32 v58, v58, v59, s[12:13]\n"
    "v_cndmask_b32 v60, v60, v61, s[12:13]\n"
    "v_cndmask_b32 v62, v62, v63, s[12:13]\n"
    "v_cndmask_b32 v60, v56, v60, s[14:15]\n"
    "v_cndmask_b32 v62, v58, v62, s[100:101]\n"
    "v_cndmask_b32 v64, v64, v65, s[12:13]\n"
    "v_cndmask_b32 v66, v66, v67, s[12:13]\n"
    "v_cndmask_b32 v68, v68, v69, s[12:13]\n"
    "v_cndmask_b32 v70, v70, v71, s[12:13]\n"
    "v_cndmask_b32 v68, v64, v68, s[14:15]\n"
    "v_cndmask_b32 v70, v66, v70, s[100:101]\n"
    "v_max_f32 v76, v44, v46\n"
    "v_max_f32 v77, v52, v54\n"
    "v_max_f32 v78, v60, v62\n"
    "v_max_f32 v79, v68, v70\n"
    "s_mov_b32 s8, 0x3eb8aa3b\n"
    "v_permlane32_swap_b32 v76, v77\n"
    "s_add_u32 m0, s93, 0x11000\n"
    "s_mov_b32 s10, 0x15800\n"
    "buffer_load_dwordx4 v12, s[28:31], s10 offen lds\n"
    "v_permlane32_swap_b32 v78, v79\n"
    "v_max_f32 v76, v76, v77\n"
    "v_max_f32 v78, v78, v79\n"
    "s_nop 1\n"
    "v_permlane16_swap_b32 v76, v78\n"
    "v_max_f32 v76, v76, v78\n"
    "v_mul_f32 v76, s8, v76\n"
    "v_mov_b32 v78, v76\n"
    "s_nop 1\n"
    "v_permlane16_swap_b32 v76, v78\n"
    "v_mov_b32 v77, v76\n"
    "v_mov_b32 v79, v78\n"
    "s_nop 0\n"
    "v_permlane32_swap_b32 v76, v77\n"
    "v_permlane32_swap_b32 v78, v79\n"
    "v_fma_f32 v44, v44, s8, -v76\n"
    "v_fma_f32 v46, v46, s8, -v76\n"
    "v_fma_f32 v52, v52, s8, -v77\n"
    "v_fma_f32 v54, v54, s8, -v77\n"
    "v_fma_f32 v60, v60, s8, -v78\n"
    "v_fma_f32 v62, v62, s8, -v78\n"
    "v_fma_f32 v68, v68, s8, -v79\n"
    "v_fma_f32 v70, v70, s8, -v79\n"
    "v_exp_f32 v44, v44\n"
    "v_exp_f32 v46, v46\n"
    "v_exp_f32 v52, v52\n"
    "v_exp_f32 v54, v54\n"
    "buffer_load_dwordx4 v11, s[28:31], s10 offen offset:1024 lds\n"
    "v_exp_f32 v60, v60\n"
    "v_exp_f32 v62, v62\n"
    "v_exp_f32 v68, v68\n"
    "v_exp_f32 v70, v70\n"
    "v_add_f32 v76, v44, v46\n"
    "v_add_f32 v77, v52, v54\n"
    "v_add_f32 v78, v60, v62\n"
    "v_add_f32 v79, v68, v70\n"
    "v_permlane32_swap_b32 v76, v77\n"
    "s_nop 0\n"
    "v_permlane32_swap_b32 v78, v79\n"
    "v_add_f32 v76, v76, v77\n"
    "v_add_f32 v78, v78, v79\n"
    "s_nop 1\n"
    "v_permlane16_swap_b32 v76, v78\n"
    "v_add_f32 v76, v76, v78\n"
    "v_rcp_f32 v76, v76\n"
    "s_nop 0\n"
    "v_mov_b32 v78, v76\n"
    "s_nop 1\n"
    "v_permlane16_swap_b32 v76, v78\n"
    "v_mov_b32 v77, v76\n"
    "v_mov_b32 v79, v78\n"
    "s_nop 0\n"
    "v_permlane32_swap_b32 v76, v77\n"
    "v_permlane32_swap_b32 v78, v79\n"
    "s_add_u32 m0, s93, 0x14000\n"
    "s_mov_b32 s10, 0x2b000\n"
    "buffer_load_dwordx4 v12, s[28:31], s10 offen lds\n"
    "s_add_u32 s95, s88, 0x8000\n"
    "v_mul_f32 v44, v44, v76\n"
    "v_mul_f32 v46, v46, v76\n"
    "v_cvt_pk_bf16_f32 v45, v44, v46\n"
    "v_perm_b32 v40, v45, v45, v125\n"
    "v_perm_b32 v41, v45, v45, v127\n"
    "v_perm_b32 v42, v45, v45, v124\n"
    "v_perm_b32 v43, v45, v45, v126\n"
    "v_mfma_f32_16x16x16_bf16 v[80:83], v[160:161], v[40:41], 0\n"
    "v_mul_f32 v52, v52, v77\n"
    "v_mul_f32 v54, v54, v77\n"
    "v_cvt_pk_bf16_f32 v53, v52, v54\n"
    "v_perm_b32 v48, v53, v53, v125\n"
    "v_perm_b32 v49, v53, v53, v127\n"
    "v_perm_b32 v50, v53, v53, v124\n"
    "v_perm_b32 v51, v53, v53, v126\n"
    "v_mfma_f32_16x16x16_bf16 v[80:83], v[162:163], v[42:43], v[80:83]\n"
    "v_mfma_f32_16x16x16_bf16 v[84:87], v[162:163], v[48:49], 0\n"
    "v_mul_f32 v60, v60, v78\n"
    "v_mul_f32 v62, v62, v78\n"
    "v_cvt_pk_bf16_f32 v61, v60, v62\n"
    "v_perm_b32 v56, v61, v61, v125\n"
    "v_perm_b32 v57, v61, v61, v127\n"
    "v_perm_b32 v58, v61, v61, v124\n"
    "v_perm_b32 v59, v61, v61, v126\n"
    "v_mfma_f32_16x16x16_bf16 v[84:87], v[164:165], v[50:51], v[84:87]\n"
    "v_mfma_f32_16x16x16_bf16 v[88:91], v[164:165], v[56:57], 0\n"
    "v_mul_f32 v68, v68, v79\n"
    "buffer_load_dwordx4 v11, s[28:31], s10 offen offset:1024 lds\n"
    "v_mul_f32 v70, v70, v79\n"
    "v_cvt_pk_bf16_f32 v69, v68, v70\n"
    "v_perm_b32 v64, v69, v69, v125\n"
    "v_perm_b32 v65, v69, v69, v127\n"
    "v_perm_b32 v66, v69, v69, v124\n"
    "v_perm_b32 v67, v69, v69, v126\n"
    "v_mfma_f32_16x16x16_bf16 v[88:91], v[166:167], v[58:59], v[88:91]\n"
    "v_mfma_f32_16x16x16_bf16 v[92:95], v[166:167], v[64:65], 0\n"
    "v_mfma_f32_16x16x16_bf16 v[92:95], v[168:169], v[66:67], v[92:95]\n"
    "v_cvt_pk_bf16_f32 v40, v80, v81\n"
    "v_cvt_pk_bf16_f32 v41, v82, v83\n"
    "v_cvt_pk_bf16_f32 v42, v84, v85\n"
    "v_cvt_pk_bf16_f32 v43, v86, v87\n"
    "s_nop 0\n"
    "v_permlane16_swap_b32 v40, v42\n"
    "v_permlane16_swap_b32 v41, v43\n"
    "buffer_store_dwordx4 v[40:43], v123, s[40:43], s88 offen sc1\n"
    "v_cvt_pk_bf16_f32 v44, v88, v89\n"
    "v_cvt_pk_bf16_f32 v45, v90, v91\n"
    "v_cvt_pk_bf16_f32 v46, v92, v93\n"
    "v_cvt_pk_bf16_f32 v47, v94, v95\n"
    "s_nop 0\n"
    "v_permlane16_swap_b32 v44, v46\n"
    "v_permlane16_swap_b32 v45, v47\n"
    "buffer_store_dwordx4 v[44:47], v123, s[40:43], s95 offen sc1\n"
    "v_mov_b32 v84, -1\n"
    "v_mov_b32 v85, -1\n"
    "v_mov_b32 v86, -1\n"
    "v_mov_b32 v87, -1\n"
    "buffer_store_dwordx4 v[84:87], v1, s[40:43], s38 offen sc1\n"
    "buffer_store_dwordx4 v[84:87], v1, s[40:43], s38 offen offset:1024 sc1\n"
    "buffer_store_dwordx4 v[84:87], v1, s[40:43], s38 offen offset:2048 sc1\n"
    "s_add_u32 m0, s93, 0x16000\n"
    "s_mov_b32 s10, 0x40800\n"
    "buffer_load_dwordx4 v12, s[28:31], s10 offen lds\n"
    "v_accvgpr_read_b32 v72, a254\n"
    "v_accvgpr_read_b32 v74, a255\n"
    "v_accvgpr_read_b32 v76, a192\n"
    "v_accvgpr_read_b32 v77, a193\n"
    "v_accvgpr_read_b32 v78, a194\n"
    "v_accvgpr_read_b32 v79, a195\n"
    "v_accvgpr_read_b32 v80, a196\n"
    "v_accvgpr_read_b32 v81, a197\n"
    "v_accvgpr_read_b32 v82, a198\n"
    "v_accvgpr_read_b32 v83, a199\n"
    "v_accvgpr_read_b32 v28, a200\n"
    "v_accvgpr_read_b32 v29, a201\n"
    "v_accvgpr_read_b32 v30, a202\n"
    "v_accvgpr_read_b32 v31, a203\n"
    "v_accvgpr_read_b32 v32, a204\n"
    "v_accvgpr_read_b32 v33, a205\n"
    "v_accvgpr_read_b32 v34, a206\n"
    "v_accvgpr_read_b32 v35, a207\n"
    "v_accvgpr_read_b32 v100, a208\n"
    "v_accvgpr_read_b32 v101, a209\n"
    "v_bfe_u32 v36, v0, 2, 2\n"
    "v_and_b32 v38, 3, v0\n"
    "v_lshlrev_b32 v38, 3, v38\n"
    "v_mul_u32_u24 v39, 0x440, v14\n"
    "v_lshl_add_u32 v39, v36, 7, v39\n"
    "v_add3_u32 v39, v39, v38, s97\n"
    "v_lshrrev_b32 v37, 1, v36\n"
    "v_xor_b32 v2, 0, v37\n"
    "v_lshl_add_u32 v2, v2, 5, v39\n"
    "v_xor_b32 v3, 1, v37\n"
    "v_lshl_add_u32 v3, v3, 5, v39\n"
    "v_xor_b32 v4, 2, v37\n"
    "buffer_load_dwordx4 v11, s[28:31], s10 offen offset:1024 lds\n"
    "v_lshl_add_u32 v4, v4, 5, v39\n"
    "v_xor_b32 v5, 3, v37\n"
    "v_lshl_add_u32 v5, v5, 5, v39\n"
    "s_waitcnt vmcnt(13)\n"
    "ds_read_b64_tr_b16 a[192:193], v2\n"
    "ds_read_b64_tr_b16 a[194:195], v4 offset:512\n"
    "ds_read_b64_tr_b16 a[196:197], v3\n"
    "ds_read_b64_tr_b16 a[198:199], v5 offset:512\n"
    "ds_read_b64_tr_b16 a[200:201], v4\n"
    "ds_read_b64_tr_b16 a[202:203], v2 offset:512\n"
    "ds_read_b64_tr_b16 a[204:205], v5\n"
    "ds_read_b64_tr_b16 a[206:207], v3 offset:512\n"
    "ds_read_b64_tr_b16 a[208:209], v2 offset:20480\n"
    "ds_read_b64_tr_b16 a[210:211], v4 offset:20992\n"
    "ds_read_b64_tr_b16 a[212:213], v3 offset:20480\n"
    "ds_read_b64_tr_b16 a[214:215], v5 offset:20992\n"
    "ds_read_b64_tr_b16 a[216:217], v4 offset:20480\n"
    "ds_read_b64_tr_b16 a[218:219], v2 offset:20992\n"
    "ds_read_b64_tr_b16 a[220:221], v5 offset:20480\n"
    "ds_read_b64_tr_b16 a[222:223], v3 offset:20992\n"
    "ds_read_b64_tr_b16 a[224:225], v2 offset:40960\n"
    "ds_read_b64_tr_b16 a[226:227], v4 offset:41472\n"
    "ds_read_b64_tr_b16 a[228:229], v3 offset:40960\n"
    "ds_read_b64_tr_b16 a[230:231], v5 offset:41472\n"
    "ds_read_b64_tr_b16 a[232:233], v4 offset:40960\n"
    "ds_read_b64_tr_b16 a[234:235], v2 offset:41472\n"
    "ds_read_b64_tr_b16 a[236:237], v5 offset:40960\n"
    "ds_read_b64_tr_b16 a[238:239], v3 offset:41472\n"
    "v_add_u32 v2, 0x20000, v2\n"
    "v_add_u32 v3, 0x20000, v3\n"
    "v_add_u32 v4, 0x20000, v4\n"
    "v_add_u32 v5, 0x20000, v5\n"
    "ds_read_b64_tr_b16 a[240:241], v2\n"
    "s_add_u32 m0, s93, 0x18000\n"
    "s_mov_b32 s10, 0x56000\n"
    "buffer_load_dwordx4 v12, s[28:31], s10 offen lds\n"
    "ds_read_b64_tr_b16 a[242:243], v4 offset:512\n"
    "ds_read_b64_tr_b16 a[244:245], v3\n"
    "ds_read_b64_tr_b16 a[246:247], v5 offset:512\n"
    "ds_read_b64_tr_b16 a[248:249], v4\n"
    "ds_read_b64_tr_b16 a[250:251], v2 offset:512\n"
    "ds_read_b64_tr_b16 a[252:253], v5\n"
    "ds_read_b64_tr_b16 a[254:255], v3 offset:512\n"
    "v_lshlrev_b32 v72, 16, v72\n"
    "v_mul_f32 v73, 0xbfb8aa3b, v72\n"
    "v_exp_f32 v73, v73\n"
    "s_nop 0\n"
    "v_add_f32 v73, 1.0, v73\n"
    "v_rcp_f32 v73, v73\n"
    "s_nop 0\n"
    "v_mul_f32 v72, v72, v73\n"
    "v_cvt_pk_bf16_f32 v72, v72, v72\n"
    "v_and_b32 v72, 0xffff0000, v72\n"
    "v_lshlrev_b32 v74, 16, v74\n"
    "v_mul_f32 v75, 0xbfb8aa3b, v74\n"
    "v_exp_f32 v75, v75\n"
    "s_nop 0\n"
    "v_add_f32 v75, 1.0, v75\n"
    "v_rcp_f32 v75, v75\n"
    "s_nop 0\n"
    "v_mul_f32 v74, v74, v75\n"
    "v_cvt_pk_bf16_f32 v74, v74, v74\n"
    "v_and_b32 v74, 0xffff0000, v74\n"
    "v_lshlrev_b32 v64, 16, v76\n"
    "v_and_b32 v65, 0xffff0000, v76\n"
    "v_lshlrev_b32 v66, 16, v77\n"
    "v_and_b32 v67, 0xffff0000, v77\n"
    "v_lshlrev_b32 v68, 16, v78\n"
    "buffer_load_dwordx4 v11, s[28:31], s10 offen offset:1024 lds\n"
    "v_and_b32 v69, 0xffff0000, v78\n"
    "v_lshlrev_b32 v70, 16, v79\n"
    "v_and_b32 v71, 0xffff0000, v79\n"
    "v_pk_mul_f32 v[48:49], v[64:65], v[72:73] op_sel_hi:[1,0]\n"
    "v_pk_mul_f32 v[50:51], v[66:67], v[72:73] op_sel_hi:[1,0]\n"
    "v_pk_mul_f32 v[52:53], v[68:69], v[72:73] op_sel_hi:[1,0]\n"
    "v_pk_mul_f32 v[54:55], v[70:71], v[72:73] op_sel_hi:[1,0]\n"
    "v_lshlrev_b32 v64, 16, v80\n"
    "v_and_b32 v65, 0xffff0000, v80\n"
    "v_lshlrev_b32 v66, 16, v81\n"
    "v_and_b32 v67, 0xffff0000, v81\n"
    "v_lshlrev_b32 v68, 16, v82\n"
    "v_and_b32 v69, 0xffff0000, v82\n"
    "v_lshlrev_b32 v70, 16, v83\n"
    "v_and_b32 v71, 0xffff0000, v83\n"
    "v_pk_fma_f32 v[48:49], v[64:65], v[74:75], v[48:49] op_sel_hi:[1,0,1]\n"
    "v_pk_fma_f32 v[50:51], v[66:67], v[74:75], v[50:51] op_sel_hi:[1,0,1]\n"
    "v_pk_fma_f32 v[52:53], v[68:69], v[74:75], v[52:53] op_sel_hi:[1,0,1]\n"
    "v_pk_fma_f32 v[54:55], v[70:71], v[74:75], v[54:55] op_sel_hi:[1,0,1]\n"
    "s_mul_i32 s9, s6, 0x200\n"
    "s_add_u32 s9, s9, 0x27000\n"
    "v_and_b32 v40, 3, v0\n"
    "v_lshlrev_b32 v40, 5, v40\n"
    "v_lshl_add_u32 v40, v14, 7, v40\n"
    "v_add_u32 v40, s9, v40\n"
    "v_and_b32 v41, 31, v0\n"
    "v_lshrrev_b32 v42, 5, v0\n"
    "v_mul_u32_u24 v42, 0x400, v42\n"
    "v_lshl_add_u32 v42, v41, 2, v42\n"
    "v_add_u32 v42, 0x27000, v42\n"
    "v_lshlrev_b32 v41, 3, v41\n"
    "v_add_f32_dpp v48, v48, v48 row_ror:4 row_mask:0xf bank_mask:0xf\n"
    "v_add_f32_dpp v49, v49, v49 row_ror:4 row_mask:0xf bank_mask:0xf\n"
    "v_add_f32_dpp v50, v50, v50 row_ror:4 row_mask:0xf bank_mask:0xf\n"
    "v_add_f32_dpp v51, v51, v51 row_ror:4 row_mask:0xf bank_mask:0xf\n"
    "v_add_f32_dpp v52, v52, v52 row_ror:4 row_mask:0xf bank_mask:0xf\n"
    "v_add_f32_dpp v53, v53, v53 row_ror:4 row_mask:0xf bank_mask:0xf\n"
    "v_add_f32_dpp v54, v54, v54 row_ror:4 row_mask:0xf bank_mask:0xf\n"
    "v_add_f32_dpp v55, v55, v55 row_ror:4 row_mask:0xf bank_mask:0xf\n"
    "v_add_f32_dpp v48, v48, v48 row_ror:8 row_mask:0xf bank_mask:0xf\n"
    "v_add_f32_dpp v49, v49, v49 row_ror:8 row_mask:0xf bank_mask:0xf\n"
    "v_add_f32_dpp v50, v50, v50 row_ror:8 row_mask:0xf bank_mask:0xf\n"
    "v_add_f32_dpp v51, v51, v51 row_ror:8 row_mask:0xf bank_mask:0xf\n"
    "v_add_f32_dpp v52, v52, v52 row_ror:8 row_mask:0xf bank_mask:0xf\n"
    "v_add_f32_dpp v53, v53, v53 row_ror:8 row_mask:0xf bank_mask:0xf\n"
    "v_add_f32_dpp v54, v54, v54 row_ror:8 row_mask:0xf bank_mask:0xf\n"
    "v_add_f32_dpp v55, v55, v55 row_ror:8 row_mask:0xf bank_mask:0xf\n"
    "s_mov_b32 exec_lo, 0xf000f\n"
    "s_mov_b32 exec_hi, 0xf000f\n"
    "ds_write_b128 v40, v[48:51]\n"
    "ds_write_b128 v40, v[52:55] offset:16\n"
    "s_mov_b64 exec, -1\n"
    "s_waitcnt vmcnt(7)\n"
    "s_waitcnt lgkmcnt(0)\n"
    "s_barrier\n"
    "s_cmp_eq_u32 s6, 0\n"
    "s_cbranch_scc0 L_fo_a\n"
    "s_lshl_b32 s7, s64, 8\n"
    "s_lshl_b32 s8, s65, 2\n"
    "s_add_u32 s7, s7, s8\n"
    "s_add_u32 s7, s7, 0x10000\n"
    "v_mov_b32 v36, s67\n"
    "s_mov_b64 exec, 1\n"
    "buffer_store_dword v36, v13, s[40:43], s7 offen sc1\n"
    "s_lshl_b32 s7, s2, 2\n"
    "s_add_u32 s7, s7, 0x1000\n"
    "buffer_store_dword v36, v13, s[40:43], s7 offen sc1\n"
    "s_mov_b64 exec, -1\n"
    "s_lshl_b32 s100, s62, 8\n"
    "s_add_u32 s100, s100, 0x10000\n"
    "v_and_b32 v37, 7, v0\n"
    "v_lshlrev_b32 v37, 2, v37\n"
    "L_fo_a:\n"
    "s_lshl_b32 s10, s94, 8\n"
    "s_add_u32 s10, s10, s96\n"
    "s_add_u32 s10, s10, 0x2000\n"
    "s_cmp_eq_u32 s6, 1\n"
    "s_cbranch_scc0 L_mod2\n"
    "ds_read_b32 v64, v42\n"
    "ds_read_b32 v65, v42 offset:128\n"
    "ds_read_b32 v66, v42 offset:256\n"
    "ds_read_b32 v67, v42 offset:384\n"
    "ds_read_b32 v68, v42 offset:512\n"
    "ds_read_b32 v69, v42 offset:640\n"
    "ds_read_b32 v70, v42 offset:768\n"
    "ds_read_b32 v71, v42 offset:896\n"
    "s_waitcnt lgkmcnt(2)\n"
    "v_pk_add_f32 v[64:65], v[64:65], v[68:69]\n"
    "s_waitcnt lgkmcnt(0)\n"
    "v_pk_add_f32 v[66:67], v[66:67], v[70:71]\n"
    "v_pk_add_f32 v[64:65], v[64:65], v[66:67]\n"
    "v_add_f32 v64, v64, v65\n"
    "v_mov_b32 v65, v64\n"
    "s_nop 1\n"
    "v_permlane32_swap_b32 v64, v65\n"
    "v_add_f32 v64, v64, v65\n"
    "v_mov_b32 v65, s67\n"
    "s_mov_b32 exec_hi, 0\n"
    "buffer_store_dwordx2 v[64:65], v41, s[40:43], s10 offen sc1\n"
    "s_mov_b64 exec, -1\n"
    "L_mod2:\n"
    "s_add_u32 m0, s93, 0\n"
    "s_mov_b32 s10, 0x6b800\n"
    "buffer_load_dwordx4 v12, s[28:31], s10 offen lds\n"
    "s_lshl_b32 s4, s6, 12\n"
    "s_mov_b32 s68, 0x5200\n"
    "s_mov_b32 s69, 0xa400\n"
    "s_mov_b32 s70, 0xf600\n"
    "v_mov_b32 v44, 0x2000000\n"
    "s_mul_i32 s8, s63, 0x60\n"
    "s_lshl_b32 s9, s63, 6\n"
    "s_add_u32 s9, s9, 0xc0\n"
    "s_cmp_lt_u32 s63, 6\n"
    "s_cselect_b32 s5, s8, s9\n"
    "s_cselect_b32 s85, 3, 2\n"
    "s_lshl_b32 s7, s6, 5\n"
    "v_lshrrev_b32 v41, 3, v0\n"
    "v_bfe_u32 v42, v0, 1, 2\n"
    "v_lshrrev_b32 v39, 1, v41\n"
    "v_xor_b32 v42, v42, v39\n"
    "v_cmp_gt_u32 vcc, s85, v42\n"
    "s_nop 1\n"
    "v_cndmask_b32 v42, v44, v42\n"
    "v_add_u32 v41, s7, v41\n"
    "v_mul_u32_u24 v41, 0xac0, v41\n"
    "v_lshl_add_u32 v41, v42, 5, v41\n"
    "v_and_b32 v38, 1, v0\n"
    "v_lshl_add_u32 v41, v38, 4, v41\n"
    "s_add_u32 s8, s5, 0x800\n"
    "v_add_u32 v10, s8, v41\n"
    "buffer_load_dwordx4 v11, s[28:31], s10 offen offset:1024 lds\n"
    "v_lshrrev_b32 v41, 2, v0\n"
    "v_and_b32 v38, 3, v0\n"
    "v_lshlrev_b32 v38, 3, v38\n"
    "v_and_b32 v42, 7, v41\n"
    "s_lshl_b32 s7, s6, 1\n"
    "s_add_u32 s8, s7, 0\n"
    "v_xor_b32 v39, s8, v42\n"
    "v_lshl_add_u32 v39, v39, 5, v38\n"
    "v_lshl_add_u32 v16, v41, 8, v39\n"
    "s_add_u32 s8, s7, 1\n"
    "v_xor_b32 v39, s8, v42\n"
    "v_lshl_add_u32 v39, v39, 5, v38\n"
    "v_lshl_add_u32 v17, v41, 8, v39\n"
    "v_bfe_u32 v42, v41, 1, 2\n"
    "v_xor_b32 v39, s6, v42\n"
    "v_lshl_add_u32 v39, v39, 5, v38\n"
    "v_lshl_add_u32 v18, v41, 7, v39\n"
    "s_lshl_b32 s7, s62, 16\n"
    "s_lshl_b32 s8, s6, 12\n"
    "s_add_u32 s7, s7, s8\n"
    "s_add_u32 s7, s7, 0x100000\n"
    "s_add_u32 s56, s87, 1\n"
    "s_and_b32 s56, s56, 3\n"
    "s_lshl_b32 s20, s56, 14\n"
    "s_add_u32 s20, s20, s7\n"
    "s_add_u32 m0, s93, 0x2000\n"
    "s_mov_b32 s10, 0xc1800\n"
    "buffer_load_dwordx4 v12, s[28:31], s10 offen lds\n"
    "s_add_u32 s8, s86, 0\n"
    "s_and_b32 s8, s8, 3\n"
    "s_lshl_b32 s8, s8, 10\n"
    "v_add_u32 v116, s8, v1\n"
    "s_add_u32 s57, s87, 2\n"
    "s_and_b32 s57, s57, 3\n"
    "s_lshl_b32 s21, s57, 14\n"
    "s_add_u32 s21, s21, s7\n"
    "s_add_u32 s8, s86, 1\n"
    "s_and_b32 s8, s8, 3\n"
    "s_lshl_b32 s8, s8, 10\n"
    "v_add_u32 v117, s8, v1\n"
    "s_add_u32 s58, s87, 3\n"
    "s_and_b32 s58, s58, 3\n"
    "s_lshl_b32 s22, s58, 14\n"
    "s_add_u32 s22, s22, s7\n"
    "s_add_u32 s8, s86, 2\n"
    "s_and_b32 s8, s8, 3\n"
    "s_lshl_b32 s8, s8, 10\n"
    "v_add_u32 v118, s8, v1\n"
    "s_add_u32 s59, s87, 4\n"
    "s_and_b32 s59, s59, 3\n"
    "s_lshl_b32 s23, s59, 14\n"
    "s_add_u32 s23, s23, s7\n"
    "s_add_u32 s8, s86, 3\n"
    "buffer_load_dwordx4 v11, s[28:31], s10 offen offset:1024 lds\n"
    "s_and_b32 s8, s8, 3\n"
    "s_lshl_b32 s8, s8, 10\n"
    "v_add_u32 v119, s8, v1\n"
    "s_mul_i32 s7, s56, 0x3000\n"
    "s_add_u32 s7, s7, 0x14800\n"
    "v_add_u32 v120, s7, v1\n"
    "s_mul_i32 s7, s57, 0x3000\n"
    "s_add_u32 s7, s7, 0x15800\n"
    "v_add_u32 v121, s7, v1\n"
    "s_mul_i32 s7, s58, 0x3000\n"
    "s_add_u32 s7, s7, 0x16800\n"
    "v_add_u32 v122, s7, v1\n"
    "s_mul_i32 s7, s87, 0x3000\n"
    "s_add_u32 s7, s7, 0x14800\n"
    "v_add_u32 v123, s7, v1\n"
    "v_lshlrev_b32 v124, 4, v14\n"
    "v_add_u32 v124, 0x27000, v124\n"
    "v_and_b32 v127, 15, v0\n"
    "v_lshlrev_b32 v125, 5, v127\n"
    "v_lshl_add_u32 v125, v14, 3, v125\n"
    "v_lshrrev_b32 v38, 5, v0\n"
    "v_mul_u32_u24 v38, 0x6000, v38\n"
    "v_and_b32 v39, 31, v0\n"
    "v_lshl_add_u32 v38, v39, 4, v38\n"
    "s_lshl_b32 s7, s63, 9\n"
    "s_add_u32 m0, s93, 0x4000\n"
    "s_mov_b32 s10, 0xd7000\n"
    "buffer_load_dwordx4 v12, s[28:31], s10 offen lds\n"
    "s_lshl_b32 s8, s6, 12\n"
    "s_add_u32 s7, s7, s8\n"
    "v_add_u32 v126, s7, v38\n"
    "v_permlane16_swap_b32 v28, v30\n"
    "v_permlane16_swap_b32 v29, v31\n"
    "v_permlane16_swap_b32 v32, v34\n"
    "v_permlane16_swap_b32 v33, v35\n"
    "v_lshlrev_b32 v20, 16, v28\n"
    "v_and_b32 v21, 0xffff0000, v28\n"
    "v_lshlrev_b32 v22, 16, v29\n"
    "v_and_b32 v23, 0xffff0000, v29\n"
    "v_lshlrev_b32 v24, 16, v30\n"
    "v_and_b32 v25, 0xffff0000, v30\n"
    "v_lshlrev_b32 v26, 16, v31\n"
    "v_and_b32 v27, 0xffff0000, v31\n"
    "v_lshlrev_b32 v28, 16, v32\n"
    "v_and_b32 v29, 0xffff0000, v32\n"
    "v_lshlrev_b32 v30, 16, v33\n"
    "v_and_b32 v31, 0xffff0000, v33\n"
    "v_lshlrev_b32 v32, 16, v34\n"
    "v_and_b32 v33, 0xffff0000, v34\n"
    "buffer_load_dwordx4 v11, s[28:31], s10 offen offset:1024 lds\n"
    "v_lshlrev_b32 v34, 16, v35\n"
    "v_and_b32 v35, 0xffff0000, v35\n"
    "v_lshlrev_b32 v104, 16, v100\n"
    "v_and_b32 v105, 0xffff0000, v100\n"
    "v_lshlrev_b32 v106, 16, v101\n"
    "v_and_b32 v107, 0xffff0000, v101\n"
    "v_lshlrev_b32 v108, 4, v127\n"
    "v_add_u32 v108, 0x27400, v108\n"
    "v_and_b32 v45, 15, v0\n"
    "v_lshlrev_b32 v45, 1, v45\n"
    "v_mov_b32 v46, 0x40000000\n"
    "v_cmp_eq_u32 vcc, 3, v14\n"
    "s_nop 1\n"
    "v_cndmask_b32 v47, v46, v45\n"
    "s_lshl_b32 s7, s63, 8\n"
    "s_lshl_b32 s8, s6, 6\n"
    "s_add_u32 s7, s7, s8\n"
    "buffer_load_ushort a180, v47, s[48:51], s7 offen\n"
    "buffer_load_ushort a181, v47, s[48:51], s7 offen offset:32\n"
    "s_lshl_b32 s8, s6, 5\n"
    "s_add_u32 s8, s8, s5\n"
    "v_add_u32 v45, s8, v47\n"
    "buffer_load_ushort a182, v45, s[48:51], 0 offen offset:2048\n"
    "s_lshl_b32 s7, s6, 11\n"
    "s_add_u32 s8, s7, 0\n"
    "v_add_u32 v45, s8, v1\n"
    "s_add_u32 s8, s7, 0x10000\n"
    "v_add_u32 v46, s8, v1\n"
    "s_add_u32 s8, s7, 0x20000\n"
    "v_add_u32 v47, s8, v1\n"
    "ds_write_b128 v45, v[176:179] offset:24576\n"
    "ds_write_b128 v45, v[180:183] offset:25600\n"
    "ds_write_b128 v45, v[184:187] offset:32768\n"
    "ds_write_b128 v45, v[188:191] offset:33792\n"
    "ds_write_b128 v45, v[192:195] offset:40960\n"
    "ds_write_b128 v45, v[196:199] offset:41984\n"
    "s_cmp_eq_u32 s6, 0\n"
    "s_cbranch_scc0 L_fo_p\n"
    "buffer_load_dword v36, v37, s[40:43], s100 offen sc1\n"
    "L_fo_p:\n"
    "ds_write_b128 v45, v[216:219] offset:49152\n"
    "ds_write_b128 v45, v[220:223] offset:50176\n"
    "ds_write_b128 v47, v[224:227]\n"
    "ds_write_b128 v47, v[228:231] offset:1024\n"
    "ds_write_b128 v47, v[232:235] offset:8192\n"
    "ds_write_b128 v47, v[236:239] offset:9216\n"
    "ds_write_b128 v47, v[240:243] offset:16384\n"
    "ds_write_b128 v47, v[244:247] offset:17408\n"
    "ds_write_b128 v46, v[248:251] offset:40960\n"
    "ds_write_b128 v46, v[252:255] offset:41984\n"
    "s_cmp_eq_u32 s6, 0\n"
    "s_cbranch_scc0 L_fo_b\n"
    "s_waitcnt vmcnt(0)\n"
    "v_cmp_ne_u32 vcc, s67, v36\n"
    "s_nop 0\n"
    "s_cbranch_vccnz L_fo\n"
    "L_fo_ok:\n"
    "L_fo_b:\n"
    "s_waitcnt vmcnt(3)\n"
    "s_waitcnt lgkmcnt(0)\n"
    "s_barrier\n"
    "s_mov_b32 s11, 0x62000\n"
    "buffer_load_dwordx4 v[92:95], v126, s[40:43], s11 offen sc1\n"
    "s_mov_b32 s11, 0x6e000\n"
    "buffer_load_dwordx4 v[96:99], v126, s[40:43], s11 offen sc1\n"
    "s_waitcnt vmcnt(2)\n"
    "buffer_load_dwordx4 v[36:39], v116, s[40:43], s20 offen sc1\n"
    "buffer_load_dwordx4 v[40:43], v117, s[40:43], s20 offen sc1\n"
    "buffer_load_dwordx4 v[44:47], v118, s[40:43], s20 offen sc1\n"
    "buffer_load_dwordx4 v[48:51], v119, s[40:43], s20 offen sc1\n"
    "buffer_load_dwordx4 v[52:55], v116, s[40:43], s21 offen sc1\n"
    "buffer_load_dwordx4 v[56:59], v117, s[40:43], s21 offen sc1\n"
    "buffer_load_dwordx4 v[60:63], v118, s[40:43], s21 offen sc1\n"
    "buffer_load_dwordx4 v[64:67], v119, s[40:43], s21 offen sc1\n"
    "ds_write_b128 v108, v[104:107]\n"
    "ds_read_b64_tr_b16 v[168:169], v16\n"
    "ds_read_b64_tr_b16 v[170:171], v16 offset:4096\n"
    "ds_read_b64_tr_b16 v[172:173], v17\n"
    "ds_read_b64_tr_b16 v[174:175], v17 offset:4096\n"
    "ds_read_b64_tr_b16 v[200:201], v16 offset:8192\n"
    "ds_read_b64_tr_b16 v[202:203], v16 offset:12288\n"
    "ds_read_b64_tr_b16 v[204:205], v17 offset:8192\n"
    "ds_read_b64_tr_b16 v[206:207], v17 offset:12288\n"
    "ds_read_b64_tr_b16 v[208:209], v16 offset:16384\n"
    "ds_read_b64_tr_b16 v[210:211], v16 offset:20480\n"
    "ds_read_b64_tr_b16 v[212:213], v17 offset:16384\n"
    "ds_read_b64_tr_b16 v[214:215], v17 offset:20480\n"
    "ds_read_b64_tr_b16 v[176:177], v16 offset:24576\n"
    "ds_read_b64_tr_b16 v[178:179], v16 offset:28672\n"
    "ds_read_b64_tr_b16 v[180:181], v17 offset:24576\n"
    "ds_read_b64_tr_b16 v[182:183], v17 offset:28672\n"
    "ds_read_b64_tr_b16 v[184:185], v16 offset:32768\n"
    "ds_read_b64_tr_b16 v[186:187], v16 offset:36864\n"
    "ds_read_b64_tr_b16 v[188:189], v17 offset:32768\n"
    "ds_read_b64_tr_b16 v[190:191], v17 offset:36864\n"
    "ds_read_b64_tr_b16 v[192:193], v16 offset:40960\n"
    "ds_read_b64_tr_b16 v[194:195], v16 offset:45056\n"
    "ds_read_b64_tr_b16 v[196:197], v17 offset:40960\n"
    "ds_read_b64_tr_b16 v[198:199], v17 offset:45056\n"
    "ds_read_b64_tr_b16 v[216:217], v16 offset:49152\n"
    "ds_read_b64_tr_b16 v[218:219], v16 offset:53248\n"
    "ds_read_b64_tr_b16 v[220:221], v17 offset:49152\n"
    "ds_read_b64_tr_b16 v[222:223], v17 offset:53248\n"
    "v_add_u32 v16, 0xf000, v16\n"
    "v_add_u32 v17, 0xf000, v17\n"
    "ds_read_b64_tr_b16 v[128:129], v16\n"
    "ds_read_b64_tr_b16 v[130:131], v16 offset:4096\n"
    "ds_read_b64_tr_b16 v[132:133], v17\n"
    "ds_read_b64_tr_b16 v[134:135], v17 offset:4096\n"
    "ds_read_b64_tr_b16 v[136:137], v16 offset:8192\n"
    "ds_read_b64_tr_b16 v[138:139], v16 offset:12288\n"
    "ds_read_b64_tr_b16 v[140:141], v17 offset:8192\n"
    "ds_read_b64_tr_b16 v[142:143], v17 offset:12288\n"
    "ds_read_b64_tr_b16 v[144:145], v16 offset:20480\n"
    "ds_read_b64_tr_b16 v[146:147], v16 offset:24576\n"
    "ds_read_b64_tr_b16 v[148:149], v17 offset:20480\n"
    "ds_read_b64_tr_b16 v[150:151], v17 offset:24576\n"
    "ds_read_b64_tr_b16 v[152:153], v16 offset:28672\n"
    "ds_read_b64_tr_b16 v[154:155], v16 offset:32768\n"
    "ds_read_b64_tr_b16 v[156:157], v17 offset:28672\n"
    "ds_read_b64_tr_b16 v[158:159], v17 offset:32768\n"
    "ds_read_b64_tr_b16 v[160:161], v16 offset:36864\n"
    "ds_read_b64_tr_b16 v[162:163], v16 offset:40960\n"
    "ds_read_b64_tr_b16 v[164:165], v17 offset:36864\n"
    "ds_read_b64_tr_b16 v[166:167], v17 offset:40960\n"
    "ds_read_b64_tr_b16 v[248:249], v16 offset:45056\n"
    "ds_read_b64_tr_b16 v[250:251], v16 offset:49152\n"
    "ds_read_b64_tr_b16 v[252:253], v17 offset:45056\n"
    "ds_read_b64_tr_b16 v[254:255], v17 offset:49152\n"
    "v_add_u32 v16, 0x11000, v16\n"
    "v_add_u32 v17, 0x11000, v17\n"
    "ds_read_b64_tr_b16 v[224:225], v16\n"
    "ds_read_b64_tr_b16 v[226:227], v16 offset:4096\n"
    "ds_read_b64_tr_b16 v[228:229], v17\n"
    "ds_read_b64_tr_b16 v[230:231], v17 offset:4096\n"
    "ds_read_b64_tr_b16 v[232:233], v16 offset:8192\n"
    "ds_read_b64_tr_b16 v[234:235], v16 offset:12288\n"
    "ds_read_b64_tr_b16 v[236:237], v17 offset:8192\n"
    "ds_read_b64_tr_b16 v[238:239], v17 offset:12288\n"
    "ds_read_b64_tr_b16 v[240:241], v16 offset:16384\n"
    "ds_read_b64_tr_b16 v[242:243], v16 offset:20480\n"
    "ds_read_b64_tr_b16 v[244:245], v17 offset:16384\n"
    "ds_read_b64_tr_b16 v[246:247], v17 offset:20480\n"
    "buffer_load_dwordx4 v[68:71], v116, s[40:43], s22 offen sc1\n"
    "buffer_load_dwordx4 v[72:75], v117, s[40:43], s22 offen sc1\n"
    "buffer_load_dwordx4 v[76:79], v118, s[40:43], s22 offen sc1\n"
    "buffer_load_dwordx4 v[80:83], v119, s[40:43], s22 offen sc1\n"
    "s_waitcnt lgkmcnt(0)\n"
    "s_barrier\n"
    "s_waitcnt vmcnt(11)\n"
    "v_mfma_f32_16x16x32_bf16 a[0:3], a[192:195], v[36:39], 0\n"
    "buffer_load_dwordx4 v[84:87], v116, s[40:43], s23 offen sc1\n"
    "s_mul_i32 s52, s6, 0x1100\n"
    "s_add_u32 s55, s52, 0x10000\n"
    "v_mfma_f32_16x16x32_bf16 a[4:7], a[196:199], v[36:39], 0\n"
    "v_bfe_u32 v112, v0, 2, 2\n"
    "v_and_b32 v114, 3, v0\n"
    "v_lshlrev_b32 v114, 3, v114\n"
    "v_mfma_f32_16x16x32_bf16 a[8:11], a[200:203], v[36:39], 0\n"
    "buffer_load_dwordx4 v[88:91], v117, s[40:43], s23 offen sc1\n"
    "v_mul_u32_u24 v115, 0x440, v14\n"
    "v_lshl_add_u32 v115, v112, 7, v115\n"
    "v_add3_u32 v115, v115, v114, s55\n"
    "v_mfma_f32_16x16x32_bf16 a[12:15], a[204:207], v[36:39], 0\n"
    "v_lshrrev_b32 v113, 1, v112\n"
    "v_xor_b32 v2, 0, v113\n"
    "v_lshl_add_u32 v2, v2, 5, v115\n"
    "s_waitcnt vmcnt(12)\n"
    "v_mfma_f32_16x16x32_bf16 a[0:3], a[208:211], v[40:43], a[0:3]\n"
    "v_xor_b32 v109, s67, v93\n"
    "v_bitop3_b32 v109, v109, v95, s67 bitop3:0xf6\n"
    "v_bitop3_b32 v109, v109, v97, s67 bitop3:0xf6\n"
    "v_bitop3_b32 v109, v109, v99, s67 bitop3:0xf6\n"
    "v_cmp_ne_u32 vcc, 0, v109\n"
    "s_nop 0\n"
    "s_cbranch_vccnz L_g3\n"
    "L_g3_ok:\n"
    "v_add_f32 v110, v92, v96\n"
    "v_add_f32 v111, v94, v98\n"
    "v_mov_b32 v92, v110\n"
    "v_mov_b32 v93, v111\n"
    "v_and_b32 v102, 31, v0\n"
    "v_lshlrev_b32 v102, 3, v102\n"
    "v_permlane32_swap_b32 v110, v92\n"
    "v_permlane32_swap_b32 v111, v93\n"
    "v_add_f32 v110, v110, v92\n"
    "v_add_f32 v111, v111, v93\n"
    "s_cmp_eq_u32 s6, 2\n"
    "s_cselect_b32 s7, 1.0, 0\n"
    "v_cvt_pk_bf16_f32 v100, v110, v111\n"
    "v_lshlrev_b32 v110, 16, v100\n"
    "v_and_b32 v111, 0xffff0000, v100\n"
    "v_add_f32 v110, s7, v110\n"
    "v_add_f32 v111, s7, v111\n"
    "v_cvt_pk_bf16_f32 v100, v110, v111\n"
    "v_lshlrev_b32 v110, 16, v100\n"
    "v_and_b32 v111, 0xffff0000, v100\n"
    "s_lshl_b32 s8, s6, 8\n"
    "s_add_u32 s7, s8, 0x27000\n"
    "v_add_u32 v103, s7, v102\n"
    "ds_write_b64 v103, v[110:111]\n"
    "v_mov_b32 v101, s67\n"
    "s_lshl_b32 s7, s62, 3\n"
    "s_add_u32 s7, s7, s63\n"
    "s_lshl_b32 s7, s7, 9\n"
    "s_add_u32 s7, s7, s8\n"
    "s_add_u32 s7, s7, 0x7ff00\n"
    "s_sub_u32 s8, s6, 1\n"
    "s_cmp_lt_u32 s8, 2\n"
    "s_cselect_b32 s8, 0, 0x40000000\n"
    "v_lshlrev_b32 v103, 20, v0\n"
    "v_and_b32 v103, 0x2000000, v103\n"
    "v_or3_b32 v102, v102, v103, s8\n"
    "buffer_store_dwordx2 v[100:101], v102, s[40:43], s7 offen sc1\n"
    "buffer_load_dwordx4 v[92:95], v118, s[40:43], s23 offen sc1\n"
    "buffer_load_dwordx4 v[96:99], v119, s[40:43], s23 offen sc1\n"
    "v_mfma_f32_16x16x32_bf16 a[4:7], a[212:215], v[40:43], a[4:7]\n"
    "v_xor_b32 v3, 1, v113\n"
    "v_lshl_add_u32 v3, v3, 5, v115\n"
    "v_xor_b32 v4, 2, v113\n"
    "v_mfma_f32_16x16x32_bf16 a[8:11], a[216:219], v[40:43], a[8:11]\n"
    "v_lshl_add_u32 v4, v4, 5, v115\n"
    "v_xor_b32 v5, 3, v113\n"
    "v_lshl_add_u32 v5, v5, 5, v115\n"
    "v_mfma_f32_16x16x32_bf16 a[12:15], a[220:223], v[40:43], a[12:15]\n"
    "v_and_b32 v114, 3, v0\n"
    "v_lshlrev_b32 v114, 3, v114\n"
    "v_bfe_u32 v112, v0, 2, 3\n"
    "s_waitcnt vmcnt(14)\n"
    "v_mfma_f32_16x16x32_bf16 a[0:3], a[224:227], v[44:47], a[0:3]\n"
    "v_lshrrev_b32 v115, 1, v14\n"
    "v_mul_u32_u24 v115, 0x440, v115\n"
    "v_lshl_add_u32 v115, v112, 7, v115\n"
    "v_mfma_f32_16x16x32_bf16 a[4:7], a[228:231], v[44:47], a[4:7]\n"
    "v_add3_u32 v115, v115, v114, s55\n"
    "v_lshrrev_b32 v113, 1, v112\n"
    "v_xor_b32 v6, 0, v113\n"
    "v_mfma_f32_16x16x32_bf16 a[8:11], a[232:235], v[44:47], a[8:11]\n"
    "v_lshl_add_u32 v6, v6, 5, v115\n"
    "v_xor_b32 v7, 1, v113\n"
    "v_lshl_add_u32 v7, v7, 5, v115\n"
    "v_mfma_f32_16x16x32_bf16 a[12:15], a[236:239], v[44:47], a[12:15]\n"
    "v_xor_b32 v8, 2, v113\n"
    "v_lshl_add_u32 v8, v8, 5, v115\n"
    "v_xor_b32 v9, 3, v113\n"
    "s_waitcnt vmcnt(13)\n"
    "v_mfma_f32_16x16x32_bf16 a[0:3], a[240:243], v[48:51], a[0:3]\n"
    "v_lshl_add_u32 v9, v9, 5, v115\n"
    "v_lshrrev_b32 v112, 3, v0\n"
    "v_lshlrev_b32 v127, 10, v112\n"
    "v_mfma_f32_16x16x32_bf16 a[4:7], a[244:247], v[48:51], a[4:7]\n"
    "s_add_u32 m0, s4, 0\n"
    "v_lshrrev_b32 v112, 1, v112\n"
    "v_bfe_u32 v113, v0, 1, 2\n"
    "v_mfma_f32_16x16x32_bf16 a[8:11], a[248:251], v[48:51], a[8:11]\n"
    "v_xor_b32 v112, v112, v113\n"
    "v_lshl_add_u32 v127, v112, 5, v127\n"
    "v_and_b32 v112, 1, v0\n"
    "v_mfma_f32_16x16x32_bf16 a[12:15], a[252:255], v[48:51], a[12:15]\n"
    "buffer_load_dwordx4 v10, s[28:31], 0 offen lds\n"
    "v_lshl_add_u32 v127, v112, 4, v127\n"
    "s_lshl_b32 s53, s63, 7\n"
    "s_waitcnt vmcnt(13)\n"
    "v_mfma_f32_16x16x32_bf16 a[16:19], a[192:195], v[52:55], 0\n"
    "v_add_u32 v127, s53, v127\n"
    "s_mul_i32 s53, s62, 0x2c000\n"
    "s_add_u32 s53, s53, 0x600000\n"
    "v_mfma_f32_16x16x32_bf16 a[20:23], a[196:199], v[52:55], 0\n"
    "s_lshr_b32 s54, s63, 1\n"
    "s_mul_i32 s54, s54, 11\n"
    "s_and_b32 s55, s63, 1\n"
    "v_mfma_f32_16x16x32_bf16 a[24:27], a[200:203], v[52:55], 0\n"
    "buffer_load_dwordx4 v10, s[28:31], s68 offen offset:1024 lds\n"
    "s_lshl_b32 s55, s55, 2\n"
    "s_add_u32 s54, s54, s55\n"
    "v_mfma_f32_16x16x32_bf16 a[28:31], a[204:207], v[52:55], 0\n"
    "ds_write_b128 v120, a[0:3]\n"
    "s_add_u32 s54, s54, s6\n"
    "s_lshl_b32 s54, s54, 10\n"
    "s_add_u32 s0, s53, s54\n"
    "s_waitcnt vmcnt(13)\n"
    "v_mfma_f32_16x16x32_bf16 a[16:19], a[208:211], v[56:59], a[16:19]\n"
    "s_lshr_b32 s54, s5, 5\n"
    "s_add_u32 s54, s54, s6\n"
    "s_lshr_b32 s55, s54, 1\n"
    "v_mfma_f32_16x16x32_bf16 a[20:23], a[212:215], v[56:59], a[20:23]\n"
    "buffer_load_dwordx4 v10, s[28:31], s69 offen offset:2048 lds\n"
    "s_mul_i32 s36, s55, 11\n"
    "s_lshr_b32 s36, s36, 5\n"
    "v_mfma_f32_16x16x32_bf16 a[24:27], a[216:219], v[56:59], a[24:27]\n"
    "ds_write_b128 v120, a[4:7] offset:1024\n"
    "s_lshl_b32 s36, s36, 3\n"
    "s_add_u32 s55, s55, s36\n"
    "v_mfma_f32_16x16x32_bf16 a[28:31], a[220:223], v[56:59], a[28:31]\n"
    "s_add_u32 s55, s55, 8\n"
    "s_lshl_b32 s55, s55, 10\n"
    "s_and_b32 s54, s54, 1\n"
    "s_waitcnt vmcnt(13)\n"
    "v_mfma_f32_16x16x32_bf16 a[16:19], a[224:227], v[60:63], a[16:19]\n"
    "buffer_load_dwordx4 v10, s[28:31], s70 offen offset:3072 lds\n"
    "s_lshl_b32 s54, s54, 9\n"
    "s_add_u32 s55, s55, s54\n"
    "s_add_u32 s1, s53, s55\n"
    "v_mfma_f32_16x16x32_bf16 a[20:23], a[228:231], v[60:63], a[20:23]\n"
    "ds_write_b128 v120, a[8:11] offset:2048\n"
    "s_lshl_b32 s53, s86, 1\n"
    "s_lshl_b32 s54, s6, 18\n"
    "v_mfma_f32_16x16x32_bf16 a[24:27], a[232:235], v[60:63], a[24:27]\n"
    "s_add_u32 s16, s53, 0\n"
    "s_and_b32 s16, s16, 7\n"
    "s_lshl_b32 s16, s16, 15\n"
    "v_mfma_f32_16x16x32_bf16 a[28:31], a[236:239], v[60:63], a[28:31]\n"
    "s_add_u32 m0, s4, 0x4000\n"
    "s_add_u32 s16, s16, s54\n"
    "s_add_u32 s17, s53, 1\n"
    "s_waitcnt vmcnt(13)\n"
    "v_mfma_f32_16x16x32_bf16 a[16:19], a[240:243], v[64:67], a[16:19]\n"
    "ds_write_b128 v120, a[12:15] offset:3072\n"
    "s_and_b32 s17, s17, 7\n"
    "s_lshl_b32 s17, s17, 15\n"
    "v_mfma_f32_16x16x32_bf16 a[20:23], a[244:247], v[64:67], a[20:23]\n"
    "s_add_u32 s17, s17, s54\n"
    "s_add_u32 s18, s53, 2\n"
    "s_and_b32 s18, s18, 7\n"
    "v_mfma_f32_16x16x32_bf16 a[24:27], a[248:251], v[64:67], a[24:27]\n"
    "s_lshl_b32 s18, s18, 15\n"
    "s_add_u32 s18, s18, s54\n"
    "s_add_u32 s19, s53, 3\n"
    "v_mfma_f32_16x16x32_bf16 a[28:31], a[252:255], v[64:67], a[28:31]\n"
    "v_add_u32 v10, 0x56000, v10\n"
    "s_and_b32 s19, s19, 7\n"
    "s_lshl_b32 s19, s19, 15\n"
    "s_add_u32 s19, s19, s54\n"
    "s_waitcnt vmcnt(12)\n"
    "v_mfma_f32_16x16x32_bf16 a[32:35], a[192:195], v[68:71], 0\n"
    "s_add_u32 s24, s53, 4\n"
    "s_and_b32 s24, s24, 7\n"
    "s_lshl_b32 s24, s24, 15\n"
    "v_mfma_f32_16x16x32_bf16 a[36:39], a[196:199], v[68:71], 0\n"
    "buffer_load_dwordx4 v10, s[28:31], 0 offen lds\n"
    "s_add_u32 s24, s24, s54\n"
    "s_add_u32 s25, s53, 5\n"
    "v_mfma_f32_16x16x32_bf16 a[40:43], a[200:203], v[68:71], 0\n"
    "s_and_b32 s25, s25, 7\n"
    "s_lshl_b32 s25, s25, 15\n"
    "s_add_u32 s25, s25, s54\n"
    "v_mfma_f32_16x16x32_bf16 a[44:47], a[204:207], v[68:71], 0\n"
    "ds_write_b128 v121, a[16:19]\n"
    "s_add_u32 s26, s53, 6\n"
    "s_and_b32 s26, s26, 7\n"
    "s_waitcnt vmcnt(12)\n"
    "v_mfma_f32_16x16x32_bf16 a[32:35], a[208:211], v[72:75], a[32:35]\n"
    "buffer_load_dwordx4 v10, s[28:31], s68 offen offset:1024 lds\n"
    "s_lshl_b32 s26, s26, 15\n"
    "s_add_u32 s26, s26, s54\n"
    "v_mfma_f32_16x16x32_bf16 a[36:39], a[212:215], v[72:75], a[36:39]\n"
    "s_add_u32 s27, s53, 7\n"
    "s_and_b32 s27, s27, 7\n"
    "s_lshl_b32 s27, s27, 15\n"
    "v_mfma_f32_16x16x32_bf16 a[40:43], a[216:219], v[72:75], a[40:43]\n"
    "ds_write_b128 v121, a[20:23] offset:1024\n"
    "s_waitcnt lgkmcnt(0)\n"
    "s_barrier\n"
    "s_add_u32 s27, s27, s54\n"
    "s_mul_i32 s53, s6, 0x18000\n"
    "v_mfma_f32_16x16x32_bf16 a[44:47], a[220:223], v[72:75], a[44:47]\n"
    "s_add_u32 s37, s53, 0x100000\n"
    "s_add_u32 s56, s53, 0x108000\n"
    "s_add_u32 s57, s53, 0x110000\n"
    "s_waitcnt vmcnt(12)\n"
    "v_mfma_f32_16x16x32_bf16 a[32:35], a[224:227], v[76:79], a[32:35]\n"
    "buffer_load_dwordx4 v10, s[28:31], s69 offen offset:2048 lds\n"
    "ds_read_b128 v[36:39], v124\n"
    "v_mfma_f32_16x16x32_bf16 a[36:39], a[228:231], v[76:79], a[36:39]\n"
    "ds_write_b128 v121, a[24:27] offset:2048\n"
    "v_mfma_f32_16x16x32_bf16 a[40:43], a[232:235], v[76:79], a[40:43]\n"
    "ds_read_b128 v[40:43], v124 offset:64\n"
    "v_mfma_f32_16x16x32_bf16 a[44:47], a[236:239], v[76:79], a[44:47]\n"
    "buffer_load_dwordx4 v10, s[28:31], s70 offen offset:3072 lds\n"
    "ds_read_b128 v[44:47], v124 offset:128\n"
    "s_waitcnt vmcnt(13)\n"
    "v_mfma_f32_16x16x32_bf16 a[32:35], a[240:243], v[80:83], a[32:35]\n"
    "ds_write_b128 v121, a[28:31] offset:3072\n"
    "v_mfma_f32_16x16x32_bf16 a[36:39], a[244:247], v[80:83], a[36:39]\n"
    "ds_read_b128 v[48:51], v124 offset:192\n"
    "ds_read_b128 v[52:55], v124 offset:512\n"
    "v_mfma_f32_16x16x32_bf16 a[40:43], a[248:251], v[80:83], a[40:43]\n"
    "s_add_u32 m0, s4, 0x8000\n"
    "v_mfma_f32_16x16x32_bf16 a[44:47], a[252:255], v[80:83], a[44:47]\n"
    "ds_read_b128 v[56:59], v124 offset:576\n"
    "s_waitcnt vmcnt(12)\n"
    "v_mfma_f32_16x16x32_bf16 a[48:51], a[192:195], v[84:87], 0\n"
    "ds_read_b128 v[60:63], v124 offset:640\n"
    "v_mfma_f32_16x16x32_bf16 a[52:55], a[196:199], v[84:87], 0\n"
    "v_add_u32 v10, 0x56000, v10\n"
    "ds_read_b128 v[64:67], v124 offset:704\n"
    "v_mfma_f32_16x16x32_bf16 a[56:59], a[200:203], v[84:87], 0\n"
    "v_mfma_f32_16x16x32_bf16 a[60:63], a[204:207], v[84:87], 0\n"
    "ds_write_b128 v122, a[32:35]\n"
    "s_waitcnt vmcnt(11)\n"
    "v_mfma_f32_16x16x32_bf16 a[48:51], a[208:211], v[88:91], a[48:51]\n"
    "buffer_load_dwordx4 v10, s[28:31], 0 offen lds\n"
    "v_mfma_f32_16x16x32_bf16 a[52:55], a[212:215], v[88:91], a[52:55]\n"
    "v_mfma_f32_16x16x32_bf16 a[56:59], a[216:219], v[88:91], a[56:59]\n"
    "ds_write_b128 v122, a[36:39] offset:1024\n"
    "v_mfma_f32_16x16x32_bf16 a[60:63], a[220:223], v[88:91], a[60:63]\n"
    "buffer_load_dwordx4 v10, s[28:31], s68 offen offset:1024 lds\n"
    "s_waitcnt vmcnt(11)\n"
    "v_mfma_f32_16x16x32_bf16 a[48:51], a[224:227], v[92:95], a[48:51]\n"
    "v_mfma_f32_16x16x32_bf16 a[52:55], a[228:231], v[92:95], a[52:55]\n"
    "ds_write_b128 v122, a[40:43] offset:2048\n"
    "v_mfma_f32_16x16x32_bf16 a[56:59], a[232:235], v[92:95], a[56:59]\n"
    "buffer_load_dwordx4 v10, s[28:31], s69 offen offset:2048 lds\n"
    "v_mfma_f32_16x16x32_bf16 a[60:63], a[236:239], v[92:95], a[60:63]\n"
    "s_waitcnt vmcnt(11)\n"
    "v_mfma_f32_16x16x32_bf16 a[48:51], a[240:243], v[96:99], a[48:51]\n"
    "ds_write_b128 v122, a[44:47] offset:3072\n"
    "v_mfma_f32_16x16x32_bf16 a[52:55], a[244:247], v[96:99], a[52:55]\n"
    "buffer_load_dwordx4 v10, s[28:31], s70 offen offset:3072 lds\n"
    "v_mfma_f32_16x16x32_bf16 a[56:59], a[248:251], v[96:99], a[56:59]\n"
    "v_mfma_f32_16x16x32_bf16 a[60:63], a[252:255], v[96:99], a[60:63]\n"
    "s_waitcnt lgkmcnt(0)\n"
    "s_barrier\n"
    "ds_read_b128 v[68:71], v123\n"
    "ds_read_b128 v[72:75], v123 offset:1024\n"
    "ds_read_b128 v[76:79], v123 offset:2048\n"
    "ds_read_b128 v[80:83], v123 offset:3072\n"
    "ds_read_b128 v[84:87], v123 offset:4096\n"
    "ds_read_b128 v[88:91], v123 offset:5120\n"
    "ds_read_b128 v[92:95], v123 offset:6144\n"
    "ds_read_b128 v[96:99], v123 offset:7168\n"
    "ds_read_b128 v[100:103], v123 offset:8192\n"
    "ds_read_b128 v[104:107], v123 offset:9216\n"
    "ds_read_b128 v[108:111], v123 offset:10240\n"
    "ds_read_b128 v[112:115], v123 offset:11264\n"
    "s_lshl_b32 s7, s62, 12\n"
    "s_lshl_b32 s8, s6, 10\n"
    "s_add_u32 s7, s7, s8\n"
    "s_add_u32 s21, s7, 0x80000\n"
    "buffer_load_dwordx4 v[116:119], v1, s[40:43], s21 offen sc1\n"
    "s_lshl_b32 s7, s62, 16\n"
    "s_lshl_b32 s8, s87, 14\n"
    "s_add_u32 s7, s7, s8\n"
    "s_lshl_b32 s8, s63, 11\n"
    "s_add_u32 s7, s7, s8\n"
    "s_add_u32 s23, s7, 0x300000\n"
    "s_waitcnt lgkmcnt(7)\n"
    "v_pk_add_f32 v[68:69], v[68:69], v[84:85]\n"
    "v_pk_add_f32 v[70:71], v[70:71], v[86:87]\n"
    "s_waitcnt lgkmcnt(6)\n"
    "v_pk_add_f32 v[72:73], v[72:73], v[88:89]\n"
    "v_pk_add_f32 v[74:75], v[74:75], v[90:91]\n"
    "s_waitcnt lgkmcnt(5)\n"
    "v_pk_add_f32 v[76:77], v[76:77], v[92:93]\n"
    "v_pk_add_f32 v[78:79], v[78:79], v[94:95]\n"
    "s_waitcnt lgkmcnt(4)\n"
    "v_pk_add_f32 v[80:81], v[80:81], v[96:97]\n"
    "v_pk_add_f32 v[82:83], v[82:83], v[98:99]\n"
    "v_accvgpr_read_b32 v84, a48\n"
    "v_accvgpr_read_b32 v85, a49\n"
    "v_accvgpr_read_b32 v86, a50\n"
    "v_accvgpr_read_b32 v87, a51\n"
    "v_accvgpr_read_b32 v88, a52\n"
    "v_accvgpr_read_b32 v89, a53\n"
    "v_accvgpr_read_b32 v90, a54\n"
    "v_accvgpr_read_b32 v91, a55\n"
    "v_accvgpr_read_b32 v92, a56\n"
    "v_accvgpr_read_b32 v93, a57\n"
    "v_accvgpr_read_b32 v94, a58\n"
    "v_accvgpr_read_b32 v95, a59\n"
    "v_accvgpr_read_b32 v96, a60\n"
    "v_accvgpr_read_b32 v97, a61\n"
    "v_accvgpr_read_b32 v98, a62\n"
    "v_accvgpr_read_b32 v99, a63\n"
    "s_waitcnt lgkmcnt(3)\n"
    "v_pk_add_f32 v[68:69], v[68:69], v[100:101]\n"
    "v_pk_add_f32 v[70:71], v[70:71], v[102:103]\n"
    "s_waitcnt lgkmcnt(2)\n"
    "v_pk_add_f32 v[72:73], v[72:73], v[104:105]\n"
    "v_pk_add_f32 v[74:75], v[74:75], v[106:107]\n"
    "s_waitcnt lgkmcnt(1)\n"
    "v_pk_add_f32 v[76:77], v[76:77], v[108:109]\n"
    "v_pk_add_f32 v[78:79], v[78:79], v[110:111]\n"
    "s_waitcnt lgkmcnt(0)\n"
    "v_pk_add_f32 v[80:81], v[80:81], v[112:113]\n"
    "v_pk_add_f32 v[82:83], v[82:83], v[114:115]\n"
    "ds_read_b128 v[100:103], v124 offset:256\n"
    "ds_read_b128 v[104:107], v124 offset:320\n"
    "ds_read_b128 v[108:111], v124 offset:384\n"
    "ds_read_b128 v[112:115], v124 offset:448\n"
    "v_pk_add_f32 v[68:69], v[68:69], v[84:85]\n"
    "v_pk_add_f32 v[70:71], v[70:71], v[86:87]\n"
    "v_pk_add_f32 v[72:73], v[72:73], v[88:89]\n"
    "v_pk_add_f32 v[74:75], v[74:75], v[90:91]\n"
    "v_pk_add_f32 v[76:77], v[76:77], v[92:93]\n"
    "v_pk_add_f32 v[78:79], v[78:79], v[94:95]\n"
    "v_pk_add_f32 v[80:81], v[80:81], v[96:97]\n"
    "v_pk_add_f32 v[82:83], v[82:83], v[98:99]\n"
    "v_pk_fma_f32 v[20:21], v[68:69], v[36:37], v[20:21]\n"
    "v_pk_fma_f32 v[22:23], v[70:71], v[38:39], v[22:23]\n"
    "v_pk_fma_f32 v[24:25], v[72:73], v[40:41], v[24:25]\n"
    "v_pk_fma_f32 v[26:27], v[74:75], v[42:43], v[26:27]\n"
    "v_pk_fma_f32 v[28:29], v[76:77], v[44:45], v[28:29]\n"
    "v_pk_fma_f32 v[30:31], v[78:79], v[46:47], v[30:31]\n"
    "v_pk_fma_f32 v[32:33], v[80:81], v[48:49], v[32:33]\n"
    "v_pk_fma_f32 v[34:35], v[82:83], v[50:51], v[34:35]\n"
    "v_pk_mul_f32 v[68:69], v[20:21], v[52:53]\n"
    "v_pk_mul_f32 v[70:71], v[22:23], v[54:55]\n"
    "v_pk_mul_f32 v[72:73], v[24:25], v[56:57]\n"
    "v_pk_mul_f32 v[74:75], v[26:27], v[58:59]\n"
    "v_cvt_pk_bf16_f32 v84, v68, v69\n"
    "v_cvt_pk_bf16_f32 v85, v70, v71\n"
    "v_cvt_pk_bf16_f32 v86, v72, v73\n"
    "v_cvt_pk_bf16_f32 v87, v74, v75\n"
    "buffer_store_dwordx4 v[84:87], v1, s[40:43], s23 offen sc1\n"
    "v_pk_mul_f32 v[76:77], v[28:29], v[60:61]\n"
    "v_pk_mul_f32 v[78:79], v[30:31], v[62:63]\n"
    "v_pk_mul_f32 v[80:81], v[32:33], v[64:65]\n"
    "v_pk_mul_f32 v[82:83], v[34:35], v[66:67]\n"
    "v_cvt_pk_bf16_f32 v88, v76, v77\n"
    "v_cvt_pk_bf16_f32 v89, v78, v79\n"
    "v_cvt_pk_bf16_f32 v90, v80, v81\n"
    "v_cvt_pk_bf16_f32 v91, v82, v83\n"
    "buffer_store_dwordx4 v[88:91], v1, s[40:43], s23 offen offset:1024 sc1\n"
    "v_pk_mul_f32 v[38:39], v[20:21], v[20:21]\n"
    "v_pk_mul_f32 v[42:43], v[68:69], v[68:69]\n"
    "v_pk_mul_f32 v[44:45], v[68:69], v[52:53]\n"
    "s_waitcnt lgkmcnt(3)\n"
    "v_pk_mul_f32 v[46:47], v[68:69], v[100:101]\n"
    "v_pk_add_f32 v[36:37], v[20:21], v[22:23]\n"
    "v_pk_fma_f32 v[38:39], v[22:23], v[22:23], v[38:39]\n"
    "v_pk_add_f32 v[40:41], v[68:69], v[70:71]\n"
    "v_pk_fma_f32 v[42:43], v[70:71], v[70:71], v[42:43]\n"
    "v_pk_fma_f32 v[44:45], v[70:71], v[54:55], v[44:45]\n"
    "v_pk_fma_f32 v[46:47], v[70:71], v[102:103], v[46:47]\n"
    "v_pk_add_f32 v[36:37], v[36:37], v[24:25]\n"
    "v_pk_fma_f32 v[38:39], v[24:25], v[24:25], v[38:39]\n"
    "v_pk_add_f32 v[40:41], v[40:41], v[72:73]\n"
    "v_pk_fma_f32 v[42:43], v[72:73], v[72:73], v[42:43]\n"
    "v_pk_fma_f32 v[44:45], v[72:73], v[56:57], v[44:45]\n"
    "s_waitcnt lgkmcnt(2)\n"
    "v_pk_fma_f32 v[46:47], v[72:73], v[104:105], v[46:47]\n"
    "v_pk_add_f32 v[36:37], v[36:37], v[26:27]\n"
    "v_pk_fma_f32 v[38:39], v[26:27], v[26:27], v[38:39]\n"
    "v_pk_add_f32 v[40:41], v[40:41], v[74:75]\n"
    "v_pk_fma_f32 v[42:43], v[74:75], v[74:75], v[42:43]\n"
    "v_pk_fma_f32 v[44:45], v[74:75], v[58:59], v[44:45]\n"
    "v_pk_fma_f32 v[46:47], v[74:75], v[106:107], v[46:47]\n"
    "v_pk_add_f32 v[36:37], v[36:37], v[28:29]\n"
    "v_pk_fma_f32 v[38:39], v[28:29], v[28:29], v[38:39]\n"
    "v_pk_add_f32 v[40:41], v[40:41], v[76:77]\n"
    "v_pk_fma_f32 v[42:43], v[76:77], v[76:77], v[42:43]\n"
    "v_pk_fma_f32 v[44:45], v[76:77], v[60:61], v[44:45]\n"
    "s_waitcnt lgkmcnt(1)\n"
    "v_pk_fma_f32 v[46:47], v[76:77], v[108:109], v[46:47]\n"
    "v_pk_add_f32 v[36:37], v[36:37], v[30:31]\n"
    "v_pk_fma_f32 v[38:39], v[30:31], v[30:31], v[38:39]\n"
    "v_pk_add_f32 v[40:41], v[40:41], v[78:79]\n"
    "v_pk_fma_f32 v[42:43], v[78:79], v[78:79], v[42:43]\n"
    "v_pk_fma_f32 v[44:45], v[78:79], v[62:63], v[44:45]\n"
    "v_pk_fma_f32 v[46:47], v[78:79], v[110:111], v[46:47]\n"
    "v_pk_add_f32 v[36:37], v[36:37], v[32:33]\n"
    "v_pk_fma_f32 v[38:39], v[32:33], v[32:33], v[38:39]\n"
    "v_pk_add_f32 v[40:41], v[40:41], v[80:81]\n"
    "v_pk_fma_f32 v[42:43], v[80:81], v[80:81], v[42:43]\n"
    "v_pk_fma_f32 v[44:45], v[80:81], v[64:65], v[44:45]\n"
    "s_waitcnt lgkmcnt(0)\n"
    "v_pk_fma_f32 v[46:47], v[80:81], v[112:113], v[46:47]\n"
    "v_pk_add_f32 v[36:37], v[36:37], v[34:35]\n"
    "v_pk_fma_f32 v[38:39], v[34:35], v[34:35], v[38:39]\n"
    "v_pk_add_f32 v[40:41], v[40:41], v[82:83]\n"
    "v_pk_fma_f32 v[42:43], v[82:83], v[82:83], v[42:43]\n"
    "v_pk_fma_f32 v[44:45], v[82:83], v[66:67], v[44:45]\n"
    "v_pk_fma_f32 v[46:47], v[82:83], v[114:115], v[46:47]\n"
    "v_add_f32 v36, v36, v37\n"
    "v_add_f32 v38, v38, v39\n"
    "v_add_f32 v40, v40, v41\n"
    "v_add_f32 v42, v42, v43\n"
    "v_add_f32 v44, v44, v45\n"
    "v_add_f32 v46, v46, v47\n"
    "v_permlane32_swap_b32 v36, v38\n"
    "v_permlane32_swap_b32 v40, v42\n"
    "v_permlane32_swap_b32 v44, v46\n"
    "v_add_f32 v36, v36, v38\n"
    "v_add_f32 v40, v40, v42\n"
    "v_add_f32 v44, v44, v46\n"
    "s_lshl_b32 s7, s62, 14\n"
    "s_lshl_b32 s8, s87, 9\n"
    "v_permlane16_swap_b32 v36, v40\n"
    "v_mov_b32 v46, v44\n"
    "v_add_f32 v36, v36, v40\n"
    "s_add_u32 s20, s7, s8\n"
    "v_permlane16_swap_b32 v44, v46\n"
    "s_add_u32 s20, s20, 0x500000\n"
    "v_add_f32 v37, v44, v46\n"
    "s_lshl_b32 s8, s63, 11\n"
    "s_add_u32 s7, s20, s8\n"
    "buffer_store_dwordx2 v[36:37], v125, s[40:43], s7 offen sc1\n"
    "v_mov_b32 v100, -1\n"
    "v_mov_b32 v101, -1\n"
    "v_mov_b32 v102, -1\n"
    "v_mov_b32 v103, -1\n"
    "buffer_store_dwordx4 v[100:103], v1, s[40:43], s38 offen offset:3072 sc1\n"
    "buffer_store_dwordx4 v[100:103], v1, s[40:43], s39 offen sc1\n"
    "v_lshlrev_b32 v104, 20, v0\n"
    "v_and_or_b32 v104, v104, s71, v1\n"
    "buffer_store_dwordx4 v[100:103], v104, s[40:43], s39 offen offset:1024 sc1\n"
    "s_lshl_b32 s7, s6, 2\n"
    "v_lshrrev_b32 v36, 1, v14\n"
    "v_lshl_add_u32 v36, v36, 1, s7\n"
    "v_bfe_u32 v37, v0, 3, 1\n"
    "v_add_u32 v36, v36, v37\n"
    "v_lshlrev_b32 v36, 6, v36\n"
    "v_and_b32 v37, 3, v0\n"
    "v_lshl_add_u32 v36, v37, 4, v36\n"
    "v_bfe_u32 v37, v0, 2, 1\n"
    "v_lshl_add_u32 v36, v37, 3, v36\n"
    "v_and_b32 v37, 1, v14\n"
    "v_xor_b32 v38, 1, v37\n"
    "v_lshl_add_u32 v38, v38, 10, v36\n"
    "v_add_u32 v38, 0x26000, v38\n"
    "s_add_u32 m0, s4, 0xc000\n"
    "v_add_u32 v10, 0x56000, v10\n"
    "buffer_load_dwordx4 v10, s[28:31], 0 offen lds\n"
    "v_lshl_add_u32 v39, v37, 10, v36\n"
    "v_add_u32 v39, 0x26800, v39\n"
    "v_cmp_eq_u32 vcc, 0, v37\n"
    "v_mov_b32 v36, 0x3f803f80\n"
    "s_nop 0\n"
    "v_cndmask_b32 v66, 0, v36\n"
    "v_mov_b32 v67, v66\n"
    "ds_write_b64 v39, v[66:67]\n"
    "buffer_load_dwordx4 v10, s[28:31], s68 offen offset:1024 lds\n"
    "buffer_load_dwordx4 v10, s[28:31], s69 offen offset:2048 lds\n"
    "buffer_load_dwordx4 v10, s[28:31], s70 offen offset:3072 lds\n"
    "s_waitcnt vmcnt(10)\n"
    "v_xor_b32 v96, s67, v117\n"
    "v_bitop3_b32 v96, v96, v119, s67 bitop3:0xf6\n"
    "v_cmp_ne_u32 vcc, 0, v96\n"
    "s_nop 0\n"
    "s_cbranch_vccnz L_g2\n"
    "L_g2_ok:\n"
    "v_mov_b32 v117, v118\n"
    "ds_write_b64 v38, v[116:117]\n"
    "v_and_b32 v36, 15, v0\n"
    "v_lshrrev_b32 v37, 2, v36\n"
    "v_and_b32 v36, 3, v36\n"
    "v_cmp_eq_u32 vcc, 0, v36\n"
    "s_nop 1\n"
    "v_cndmask_b32 v37, 3, v37\n"
    "v_lshlrev_b32 v37, 10, v37\n"
    "v_lshl_add_u32 v37, v14, 4, v37\n"
    "v_add_u32 v42, 0x26000, v37\n"
    "v_accvgpr_read_b32 v44, a180\n"
    "v_mov_b32 v45, 0\n"
    "v_mov_b32 v46, 0\n"
    "v_mov_b32 v47, 0\n"
    "v_lshlrev_b32 v44, 16, v44\n"
    "v_accvgpr_read_b32 v48, a181\n"
    "v_mov_b32 v49, 0\n"
    "v_mov_b32 v50, 0\n"
    "v_mov_b32 v51, 0\n"
    "v_lshlrev_b32 v48, 16, v48\n"
    "v_accvgpr_read_b32 v52, a182\n"
    "v_mov_b32 v53, 0\n"
    "v_mov_b32 v54, 0\n"
    "v_mov_b32 v55, 0\n"
    "v_lshlrev_b32 v52, 16, v52\n"
    "s_waitcnt lgkmcnt(0)\n"
    "s_barrier\n"
    "ds_read_b128 v[60:63], v42\n"
    "ds_read_b128 v[64:67], v42 offset:64\n"
    "ds_read_b128 v[68:71], v42 offset:128\n"
    "ds_read_b128 v[72:75], v42 offset:192\n"
    "ds_read_b128 v[76:79], v42 offset:256\n"
    "ds_read_b128 v[80:83], v42 offset:320\n"
    "ds_read_b128 v[84:87], v42 offset:384\n"
    "ds_read_b128 v[88:91], v42 offset:448\n"
    "ds_read_b128 v[92:95], v42 offset:512\n"
    "ds_read_b128 v[96:99], v42 offset:576\n"
    "ds_read_b128 v[100:103], v42 offset:640\n"
    "ds_read_b128 v[104:107], v42 offset:704\n"
    "ds_read_b128 v[108:111], v42 offset:768\n"
    "ds_read_b128 v[112:115], v42 offset:832\n"
    "s_waitcnt lgkmcnt(13)\n"
    "v_mfma_f32_16x16x32_bf16 v[44:47], v[60:63], v[128:131], v[44:47]\n"
    "v_mfma_f32_16x16x32_bf16 v[48:51], v[60:63], v[132:135], v[48:51]\n"
    "v_mfma_f32_16x16x32_bf16 v[56:59], v[60:63], v[60:63], 0\n"
    "s_add_u32 m0, s52, 0x21000\n"
    "s_waitcnt lgkmcnt(12)\n"
    "v_mfma_f32_16x16x32_bf16 v[44:47], v[64:67], v[136:139], v[44:47]\n"
    "v_mfma_f32_16x16x32_bf16 v[48:51], v[64:67], v[140:143], v[48:51]\n"
    "v_mfma_f32_16x16x32_bf16 v[56:59], v[64:67], v[64:67], v[56:59]\n"
    "s_waitcnt lgkmcnt(11)\n"
    "v_mfma_f32_16x16x32_bf16 v[44:47], v[68:71], v[144:147], v[44:47]\n"
    "v_mfma_f32_16x16x32_bf16 v[48:51], v[68:71], v[148:151], v[48:51]\n"
    "v_add_u32 v43, s16, v127\n"
    "v_mfma_f32_16x16x32_bf16 v[56:59], v[68:71], v[68:71], v[56:59]\n"
    "s_waitcnt lgkmcnt(10)\n"
    "v_mfma_f32_16x16x32_bf16 v[44:47], v[72:75], v[152:155], v[44:47]\n"
    "v_mfma_f32_16x16x32_bf16 v[48:51], v[72:75], v[156:159], v[48:51]\n"
    "v_mfma_f32_16x16x32_bf16 v[56:59], v[72:75], v[72:75], v[56:59]\n"
    "s_waitcnt lgkmcnt(9)\n"
    "v_mfma_f32_16x16x32_bf16 v[44:47], v[76:79], v[160:163], v[44:47]\n"
    "v_mfma_f32_16x16x32_bf16 v[48:51], v[76:79], v[164:167], v[48:51]\n"
    "buffer_load_dwordx4 v43, s[32:35], 0 offen lds\n"
    "v_mfma_f32_16x16x32_bf16 v[56:59], v[76:79], v[76:79], v[56:59]\n"
    "s_waitcnt lgkmcnt(8)\n"
    "v_mfma_f32_16x16x32_bf16 v[44:47], v[80:83], v[168:171], v[44:47]\n"
    "v_mfma_f32_16x16x32_bf16 v[48:51], v[80:83], v[172:175], v[48:51]\n"
    "v_mfma_f32_16x16x32_bf16 v[56:59], v[80:83], v[80:83], v[56:59]\n"
    "s_waitcnt lgkmcnt(7)\n"
    "v_mfma_f32_16x16x32_bf16 v[44:47], v[84:87], v[176:179], v[44:47]\n"
    "buffer_load_dwordx4 v43, s[32:35], s89 offen offset:1088 lds\n"
    "v_mfma_f32_16x16x32_bf16 v[48:51], v[84:87], v[180:183], v[48:51]\n"
    "v_mfma_f32_16x16x32_bf16 v[56:59], v[84:87], v[84:87], v[56:59]\n"
    "s_waitcnt lgkmcnt(6)\n"
    "v_mfma_f32_16x16x32_bf16 v[44:47], v[88:91], v[184:187], v[44:47]\n"
    "v_mfma_f32_16x16x32_bf16 v[48:51], v[88:91], v[188:191], v[48:51]\n"
    "v_mfma_f32_16x16x32_bf16 v[56:59], v[88:91], v[88:91], v[56:59]\n"
    "buffer_load_dwordx4 v43, s[32:35], s90 offen offset:2176 lds\n"
    "s_waitcnt lgkmcnt(5)\n"
    "v_mfma_f32_16x16x32_bf16 v[44:47], v[92:95], v[192:195], v[44:47]\n"
    "v_mfma_f32_16x16x32_bf16 v[48:51], v[92:95], v[196:199], v[48:51]\n"
    "v_mfma_f32_16x16x32_bf16 v[56:59], v[92:95], v[92:95], v[56:59]\n"
    "s_waitcnt lgkmcnt(4)\n"
    "v_mfma_f32_16x16x32_bf16 v[44:47], v[96:99], v[200:203], v[44:47]\n"
    "v_mfma_f32_16x16x32_bf16 v[48:51], v[96:99], v[204:207], v[48:51]\n"
    "v_mfma_f32_16x16x32_bf16 v[56:59], v[96:99], v[96:99], v[56:59]\n"
    "buffer_load_dwordx4 v43, s[32:35], s91 offen offset:3264 lds\n"
    "s_waitcnt lgkmcnt(3)\n"
    "v_mfma_f32_16x16x32_bf16 v[44:47], v[100:103], v[208:211], v[44:47]\n"
    "v_mfma_f32_16x16x32_bf16 v[48:51], v[100:103], v[212:215], v[48:51]\n"
    "v_mfma_f32_16x16x32_bf16 v[56:59], v[100:103], v[100:103], v[56:59]\n"
    "s_waitcnt vmcnt(4)\n"
    "s_waitcnt lgkmcnt(0)\n"
    "s_barrier\n"
    "s_cmp_eq_u32 s6, 0\n"
    "s_cbranch_scc0 L_fu_a\n"
    "s_lshl_b32 s7, s62, 8\n"
    "s_lshl_b32 s8, s63, 2\n"
    "s_add_u32 s7, s7, s8\n"
    "s_add_u32 s7, s7, 0x12000\n"
    "v_mov_b32 v40, s67\n"
    "s_mov_b64 exec, 1\n"
    "buffer_store_dword v40, v13, s[40:43], s7 offen sc1\n"
    "s_mov_b64 exec, -1\n"
    "s_lshl_b32 s100, s62, 8\n"
    "s_add_u32 s100, s100, 0x12000\n"
    "v_and_b32 v41, 7, v0\n"
    "v_lshlrev_b32 v41, 2, v41\n"
    "L_fu_a:\n"
    "s_add_u32 m0, s52, 0x10000\n"
    "v_add_u32 v43, s17, v127\n"
    "buffer_load_dwordx4 v43, s[32:35], 0 offen lds\n"
    "ds_read_b64_tr_b16 a[0:1], v18\n"
    "ds_read_b64_tr_b16 a[2:3], v18 offset:2048\n"
    "ds_read_b64_tr_b16 a[4:5], v18 offset:4096\n"
    "ds_read_b64_tr_b16 a[6:7], v18 offset:6144\n"
    "ds_read_b64_tr_b16 a[8:9], v18 offset:8192\n"
    "ds_read_b64_tr_b16 a[10:11], v18 offset:10240\n"
    "ds_read_b64_tr_b16 a[12:13], v18 offset:12288\n"
    "ds_read_b64_tr_b16 a[14:15], v18 offset:14336\n"
    "ds_read_b64_tr_b16 a[16:17], v18 offset:16384\n"
    "ds_read_b64_tr_b16 a[18:19], v18 offset:18432\n"
    "ds_read_b64_tr_b16 a[20:21], v18 offset:20480\n"
    "ds_read_b64_tr_b16 a[22:23], v18 offset:22528\n"
    "ds_read_b64_tr_b16 a[24:25], v18 offset:24576\n"
    "ds_read_b64_tr_b16 a[26:27], v18 offset:26624\n"
    "ds_read_b64_tr_b16 a[28:29], v18 offset:28672\n"
    "ds_read_b64_tr_b16 a[30:31], v18 offset:30720\n"
    "ds_read_b64_tr_b16 a[32:33], v18 offset:32768\n"
    "ds_read_b64_tr_b16 a[34:35], v18 offset:34816\n"
    "ds_read_b64_tr_b16 a[36:37], v18 offset:36864\n"
    "ds_read_b64_tr_b16 a[38:39], v18 offset:38912\n"
    "buffer_load_dwordx4 v43, s[32:35], s89 offen offset:1088 lds\n"
    "ds_read_b64_tr_b16 a[40:41], v18 offset:40960\n"
    "ds_read_b64_tr_b16 a[42:43], v18 offset:43008\n"
    "ds_read_b64_tr_b16 a[44:45], v18 offset:45056\n"
    "ds_read_b64_tr_b16 a[46:47], v18 offset:47104\n"
    "ds_read_b64_tr_b16 a[48:49], v18 offset:49152\n"
    "ds_read_b64_tr_b16 a[50:51], v18 offset:51200\n"
    "ds_read_b64_tr_b16 a[52:53], v18 offset:53248\n"
    "ds_read_b64_tr_b16 a[54:55], v18 offset:55296\n"
    "ds_read_b64_tr_b16 a[56:57], v18 offset:57344\n"
    "ds_read_b64_tr_b16 a[58:59], v18 offset:59392\n"
    "ds_read_b64_tr_b16 a[60:61], v18 offset:61440\n"
    "ds_read_b64_tr_b16 a[62:63], v18 offset:63488\n"
    "s_waitcnt lgkmcnt(15)\n"
    "v_mfma_f32_16x16x32_bf16 v[52:55], v[60:63], a[0:3], v[52:55]\n"
    "ds_read_b128 v[60:63], v42 offset:896\n"
    "s_waitcnt lgkmcnt(15)\n"
    "v_mfma_f32_16x16x32_bf16 v[52:55], v[64:67], a[4:7], v[52:55]\n"
    "v_and_b32 v116, 15, v0\n"
    "ds_read_b128 v[64:67], v42 offset:960\n"
    "s_waitcnt lgkmcnt(15)\n"
    "v_mfma_f32_16x16x32_bf16 v[52:55], v[68:71], a[8:11], v[52:55]\n"
    "buffer_load_dwordx4 v43, s[32:35], s90 offen offset:2176 lds\n"
    "s_waitcnt lgkmcnt(15)\n"
    "v_mfma_f32_16x16x32_bf16 v[52:55], v[72:75], a[12:15], v[52:55]\n"
    "v_lshlrev_b32 v116, 4, v116\n"
    "s_waitcnt lgkmcnt(15)\n"
    "v_mfma_f32_16x16x32_bf16 v[52:55], v[76:79], a[16:19], v[52:55]\n"
    "v_lshrrev_b32 v117, 1, v14\n"
    "s_waitcnt lgkmcnt(15)\n"
    "v_mfma_f32_16x16x32_bf16 v[52:55], v[80:83], a[20:23], v[52:55]\n"
    "v_lshl_add_u32 v116, v117, 8, v116\n"
    "s_waitcnt lgkmcnt(15)\n"
    "v_mfma_f32_16x16x32_bf16 v[52:55], v[84:87], a[24:27], v[52:55]\n"
    "s_waitcnt lgkmcnt(15)\n"
    "v_mfma_f32_16x16x32_bf16 v[52:55], v[88:91], a[28:31], v[52:55]\n"
    "s_sub_u32 s53, s85, s6\n"
    "s_waitcnt lgkmcnt(15)\n"
    "v_mfma_f32_16x16x32_bf16 v[52:55], v[92:95], a[32:35], v[52:55]\n"
    "s_sub_u32 s53, s53, 1\n"
    "s_waitcnt lgkmcnt(14)\n"
    "v_mfma_f32_16x16x32_bf16 v[52:55], v[96:99], a[36:39], v[52:55]\n"
    "buffer_load_dwordx4 v43, s[32:35], s91 offen offset:3264 lds\n"
    "s_and_b32 s53, s53, 0x40000000\n"
    "s_waitcnt lgkmcnt(12)\n"
    "v_mfma_f32_16x16x32_bf16 v[52:55], v[100:103], a[40:43], v[52:55]\n"
    "v_mfma_f32_16x16x32_bf16 v[44:47], v[104:107], v[216:219], v[44:47]\n"
    "v_or_b32 v116, s53, v116\n"
    "v_mfma_f32_16x16x32_bf16 v[48:51], v[104:107], v[220:223], v[48:51]\n"
    "v_and_b32 v117, 1, v14\n"
    "v_mfma_f32_16x16x32_bf16 v[56:59], v[104:107], v[104:107], v[56:59]\n"
    "s_waitcnt lgkmcnt(10)\n"
    "v_mfma_f32_16x16x32_bf16 v[52:55], v[104:107], a[44:47], v[52:55]\n"
    "v_add3_u32 v118, v117, s61, 0\n"
    "v_mfma_f32_16x16x32_bf16 v[44:47], v[108:111], v[224:227], v[44:47]\n"
    "v_and_b32 v118, 3, v118\n"
    "v_mfma_f32_16x16x32_bf16 v[48:51], v[108:111], v[228:231], v[48:51]\n"
    "v_mul_u32_u24 v118, 0xb000, v118\n"
    "s_add_u32 m0, s52, 0x14400\n"
    "v_add_u32 v43, s18, v127\n"
    "buffer_load_dwordx4 v43, s[32:35], 0 offen lds\n"
    "v_mfma_f32_16x16x32_bf16 v[56:59], v[108:111], v[108:111], v[56:59]\n"
    "s_waitcnt lgkmcnt(8)\n"
    "v_mfma_f32_16x16x32_bf16 v[52:55], v[108:111], a[48:51], v[52:55]\n"
    "v_add_u32 v126, v116, v118\n"
    "v_mfma_f32_16x16x32_bf16 v[44:47], v[112:115], v[232:235], v[44:47]\n"
    "v_add3_u32 v118, v117, s61, 2\n"
    "v_mfma_f32_16x16x32_bf16 v[48:51], v[112:115], v[236:239], v[48:51]\n"
    "s_cmp_eq_u32 s6, 0\n"
    "s_cbranch_scc0 L_fu_p\n"
    "buffer_load_dword v40, v41, s[40:43], s100 offen sc1\n"
    "L_fu_p:\n"
    "v_mfma_f32_16x16x32_bf16 v[56:59], v[112:115], v[112:115], v[56:59]\n"
    "v_and_b32 v118, 3, v118\n"
    "s_waitcnt lgkmcnt(6)\n"
    "v_mfma_f32_16x16x32_bf16 v[52:55], v[112:115], a[52:55], v[52:55]\n"
    "v_mul_u32_u24 v118, 0xb000, v118\n"
    "s_waitcnt lgkmcnt(1)\n"
    "v_mfma_f32_16x16x32_bf16 v[44:47], v[60:63], v[240:243], v[44:47]\n"
    "v_add_u32 v120, v116, v118\n"
    "v_mfma_f32_16x16x32_bf16 v[48:51], v[60:63], v[244:247], v[48:51]\n"
    "buffer_load_dwordx4 v43, s[32:35], s89 offen offset:1088 lds\n"
    "v_mfma_f32_16x16x32_bf16 v[56:59], v[60:63], v[60:63], v[56:59]\n"
    "s_lshl_b32 s53, s62, 16\n"
    "v_mfma_f32_16x16x32_bf16 v[52:55], v[60:63], a[56:59], v[52:55]\n"
    "s_lshl_b32 s54, s6, 12\n"
    "s_waitcnt lgkmcnt(0)\n"
    "v_mfma_f32_16x16x32_bf16 v[44:47], v[64:67], v[248:251], v[44:47]\n"
    "s_add_u32 s53, s53, s54\n"
    "v_mfma_f32_16x16x32_bf16 v[48:51], v[64:67], v[252:255], v[48:51]\n"
    "v_mfma_f32_16x16x32_bf16 v[56:59], v[64:67], v[64:67], v[56:59]\n"
    "s_add_u32 s22, s53, 0x300000\n"
    "v_mfma_f32_16x16x32_bf16 v[52:55], v[64:67], a[60:63], v[52:55]\n"
    "s_nop 0\n"
    "s_nop 0\n"
    "s_nop 0\n"
    "buffer_load_dwordx4 v43, s[32:35], s90 offen offset:2176 lds\n"
    "s_nop 0\n"
    "s_nop 0\n"
    "s_nop 0\n"
    "s_nop 0\n"
    "s_nop 0\n"
    "s_nop 0\n"
    "s_nop 0\n"
    "s_nop 0\n"
    "s_nop 0\n"
    "s_nop 0\n"
    "s_nop 0\n"
    "s_nop 0\n"
    "s_nop 0\n"
    "s_nop 0\n"
    "s_nop 0\n"
    "s_nop 0\n"
    "s_nop 0\n"
    "buffer_load_dwordx4 v43, s[32:35], s91 offen offset:3264 lds\n"
    "v_readlane_b32 s68, v56, 0\n"
    "v_readlane_b32 s69, v56, 4\n"
    "v_readlane_b32 s70, v56, 8\n"
    "v_readlane_b32 s71, v56, 20\n"
    "v_readlane_b32 s72, v56, 24\n"
    "v_mov_b32 v36, v44\n"
    "v_mov_b32 v37, v48\n"
    "v_mov_b32 v38, v52\n"
    "v_add_u32 v39, 0x26000, v1\n"
    "s_lshl_b32 s7, s2, 2\n"
    "s_add_u32 s7, s7, s6\n"
    "s_cmp_eq_u32 s7, 3\n"
    "s_cbranch_scc0 L_nochk\n"
    "s_mov_b32 s7, 0x1000\n"
    "buffer_load_dwordx4 v[116:119], v1, s[40:43], s7 offen sc1\n"
    "s_waitcnt vmcnt(0)\n"
    "v_xor_b32 v116, s67, v116\n"
    "v_bitop3_b32 v116, v116, v117, s67 bitop3:0xf6\n"
    "v_bitop3_b32 v116, v116, v118, s67 bitop3:0xf6\n"
    "v_bitop3_b32 v116, v116, v119, s67 bitop3:0xf6\n"
    "v_cmp_ne_u32 vcc, 0, v116\n"
    "s_nop 0\n"
    "s_cbranch_vccnz L_bump\n"
    "L_bump_ok:\n"
    "v_mov_b32 v116, s67\n"
    "s_mov_b64 exec, 1\n"
    "buffer_store_dword v116, v13, s[40:43], 0 offen sc1\n"
    "s_mov_b64 exec, -1\n"
    "L_nochk:\n"
    "s_cmp_eq_u32 s6, 0\n"
    "s_cbranch_scc0 L_fu_b\n"
    "s_waitcnt vmcnt(3)\n"
    "v_cmp_ne_u32 vcc, s67, v40\n"
    "s_nop 0\n"
    "s_cbranch_vccnz L_fu\n"
    "L_fu_ok:\n"
    "L_fu_b:\n"
    "s_barrier\n"
    "s_lshl_b32 s8, s6, 12\n"
    "s_add_u32 m0, s8, 0\n"
    "s_add_u32 s7, s22, 0\n"
    "buffer_load_dwordx4 v1, s[40:43], s7 offen sc1 lds\n"
    "buffer_load_dwordx4 v1, s[40:43], s7 offen offset:1024 sc1 lds\n"
    "buffer_load_dwordx4 v1, s[40:43], s7 offen offset:2048 sc1 lds\n"
    "buffer_load_dwordx4 v1, s[40:43], s7 offen offset:3072 sc1 lds\n"
    "buffer_load_dwordx2 v[92:93], v125, s[40:43], s20 offen sc1\n"
    "buffer_load_dwordx2 v[94:95], v125, s[40:43], s20 offen offset:2048 sc1\n"
    "s_add_u32 s20, s20, 0x1000\n"
    "buffer_load_dwordx2 v[96:97], v125, s[40:43], s20 offen sc1\n"
    "buffer_load_dwordx2 v[98:99], v125, s[40:43], s20 offen offset:2048 sc1\n"
    "s_add_u32 s20, s20, 0x1000\n"
    "buffer_load_dwordx2 v[100:101], v125, s[40:43], s20 offen sc1\n"
    "buffer_load_dwordx2 v[102:103], v125, s[40:43], s20 offen offset:2048 sc1\n"
    "s_add_u32 s20, s20, 0x1000\n"
    "buffer_load_dwordx2 v[104:105], v125, s[40:43], s20 offen sc1\n"
    "buffer_load_dwordx2 v[106:107], v125, s[40:43], s20 offen offset:2048 sc1\n"
    "s_waitcnt vmcnt(20)\n"
    "v_add_u32 v6, 0xcc00, v6\n"
    "v_add_u32 v7, 0xcc00, v7\n"
    "v_add_u32 v8, 0xcc00, v8\n"
    "v_add_u32 v9, 0xcc00, v9\n"
    "ds_read_b64_tr_b16 a[64:65], v6 offset:17408\n"
    "ds_read_b64_tr_b16 a[66:67], v6 offset:19584\n"
    "ds_read_b64_tr_b16 a[68:69], v7 offset:17408\n"
    "ds_read_b64_tr_b16 a[70:71], v7 offset:19584\n"
    "ds_read_b64_tr_b16 a[72:73], v8 offset:17408\n"
    "ds_read_b64_tr_b16 a[74:75], v8 offset:19584\n"
    "ds_read_b64_tr_b16 a[76:77], v9 offset:17408\n"
    "ds_read_b64_tr_b16 a[78:79], v9 offset:19584\n"
    "s_waitcnt vmcnt(16)\n"
    "v_add_u32 v6, 0xffff3400, v6\n"
    "v_add_u32 v7, 0xffff3400, v7\n"
    "v_add_u32 v8, 0xffff3400, v8\n"
    "v_add_u32 v9, 0xffff3400, v9\n"
    "ds_read_b64_tr_b16 a[80:81], v6\n"
    "ds_read_b64_tr_b16 a[82:83], v6 offset:2176\n"
    "ds_read_b64_tr_b16 a[84:85], v7\n"
    "ds_read_b64_tr_b16 a[86:87], v7 offset:2176\n"
    "ds_read_b64_tr_b16 a[88:89], v8\n"
    "ds_read_b64_tr_b16 a[90:91], v8 offset:2176\n"
    "ds_read_b64_tr_b16 a[92:93], v9\n"
    "ds_read_b64_tr_b16 a[94:95], v9 offset:2176\n"
    "s_waitcnt vmcnt(12)\n"
    "ds_read_b64_tr_b16 a[96:97], v6 offset:17408\n"
    "ds_read_b64_tr_b16 a[98:99], v6 offset:19584\n"
    "ds_read_b64_tr_b16 a[100:101], v7 offset:17408\n"
    "ds_read_b64_tr_b16 a[102:103], v7 offset:19584\n"
    "ds_read_b64_tr_b16 a[104:105], v8 offset:17408\n"
    "ds_read_b64_tr_b16 a[106:107], v8 offset:19584\n"
    "ds_read_b64_tr_b16 a[108:109], v9 offset:17408\n"
    "ds_read_b64_tr_b16 a[110:111], v9 offset:19584\n"
    "s_add_u32 m0, s8, 0x4000\n"
    "s_add_u32 s7, s22, 0x4000\n"
    "buffer_load_dwordx4 v1, s[40:43], s7 offen sc1 lds\n"
    "buffer_load_dwordx4 v1, s[40:43], s7 offen offset:1024 sc1 lds\n"
    "buffer_load_dwordx4 v1, s[40:43], s7 offen offset:2048 sc1 lds\n"
    "buffer_load_dwordx4 v1, s[40:43], s7 offen offset:3072 sc1 lds\n"
    "s_waitcnt vmcnt(12)\n"
    "s_waitcnt lgkmcnt(0)\n"
    "s_barrier\n"
    "ds_read_b128 v[44:47], v1\n"
    "ds_read_b128 v[48:51], v1 offset:1024\n"
    "ds_read_b128 v[52:55], v1 offset:2048\n"
    "ds_read_b128 v[56:59], v1 offset:3072\n"
    "ds_read_b128 v[60:63], v1 offset:4096\n"
    "ds_read_b128 v[64:67], v1 offset:5120\n"
    "s_waitcnt lgkmcnt(4)\n"
    "v_mfma_f32_16x16x32_bf16 v[68:71], v[128:131], v[44:47], 0\n"
    "s_add_u32 m0, s8, 0x8000\n"
    "v_mfma_f32_16x16x32_bf16 v[72:75], v[132:135], v[44:47], 0\n"
    "v_mfma_f32_16x16x32_bf16 v[76:79], a[0:3], v[44:47], 0\n"
    "s_add_u32 s7, s22, 0x8000\n"
    "ds_read_b128 v[44:47], v1 offset:6144\n"
    "v_mfma_f32_16x16x32_bf16 v[68:71], v[136:139], v[48:51], v[68:71]\n"
    "v_mfma_f32_16x16x32_bf16 v[72:75], v[140:143], v[48:51], v[72:75]\n"
    "buffer_load_dwordx4 v1, s[40:43], s7 offen sc1 lds\n"
    "v_mfma_f32_16x16x32_bf16 v[76:79], a[4:7], v[48:51], v[76:79]\n"
    "ds_read_b128 v[48:51], v1 offset:7168\n"
    "s_waitcnt lgkmcnt(4)\n"
    "v_mfma_f32_16x16x32_bf16 v[68:71], v[144:147], v[52:55], v[68:71]\n"
    "s_waitcnt vmcnt(11)\n"
    "v_add_f32 v92, v92, v94\n"
    "v_add_f32 v93, v93, v95\n"
    "v_mfma_f32_16x16x32_bf16 v[72:75], v[148:151], v[52:55], v[72:75]\n"
    "s_waitcnt vmcnt(10)\n"
    "v_add_f32 v92, v92, v96\n"
    "v_add_f32 v93, v93, v97\n"
    "buffer_load_dwordx4 v1, s[40:43], s7 offen offset:1024 sc1 lds\n"
    "v_mfma_f32_16x16x32_bf16 v[76:79], a[8:11], v[52:55], v[76:79]\n"
    "s_waitcnt vmcnt(10)\n"
    "v_add_f32 v92, v92, v98\n"
    "v_add_f32 v93, v93, v99\n"
    "ds_read_b128 v[52:55], v1 offset:8192\n"
    "v_mfma_f32_16x16x32_bf16 v[68:71], v[152:155], v[56:59], v[68:71]\n"
    "s_waitcnt vmcnt(9)\n"
    "v_add_f32 v92, v92, v100\n"
    "v_add_f32 v93, v93, v101\n"
    "v_mfma_f32_16x16x32_bf16 v[72:75], v[156:159], v[56:59], v[72:75]\n"
    "s_waitcnt vmcnt(8)\n"
    "v_add_f32 v92, v92, v102\n"
    "v_add_f32 v93, v93, v103\n"
    "v_mfma_f32_16x16x32_bf16 v[76:79], a[12:15], v[56:59], v[76:79]\n"
    "s_waitcnt vmcnt(7)\n"
    "v_add_f32 v92, v92, v104\n"
    "ds_read_b128 v[56:59], v1 offset:9216\n"
    "s_waitcnt lgkmcnt(4)\n"
    "v_mfma_f32_16x16x32_bf16 v[68:71], v[160:163], v[60:63], v[68:71]\n"
    "v_add_f32 v93, v93, v105\n"
    "s_waitcnt vmcnt(6)\n"
    "v_add_f32 v92, v92, v106\n"
    "v_mfma_f32_16x16x32_bf16 v[72:75], v[164:167], v[60:63], v[72:75]\n"
    "v_add_f32 v93, v93, v107\n"
    "v_mov_b32 v94, v92\n"
    "v_mfma_f32_16x16x32_bf16 v[76:79], a[16:19], v[60:63], v[76:79]\n"
    "v_mov_b32 v97, v93\n"
    "ds_read_b128 v[60:63], v1 offset:10240\n"
    "v_mfma_f32_16x16x32_bf16 v[68:71], v[168:171], v[64:67], v[68:71]\n"
    "s_mov_b32 s9, 0x3b000000\n"
    "buffer_load_dwordx4 v1, s[40:43], s7 offen offset:2048 sc1 lds\n"
    "v_mfma_f32_16x16x32_bf16 v[72:75], v[172:175], v[64:67], v[72:75]\n"
    "v_permlane16_swap_b32 v92, v94\n"
    "v_mfma_f32_16x16x32_bf16 v[76:79], a[20:23], v[64:67], v[76:79]\n"
    "v_permlane32_swap_b32 v93, v97\n"
    "ds_read_b128 v[64:67], v1 offset:11264\n"
    "s_waitcnt lgkmcnt(4)\n"
    "v_mfma_f32_16x16x32_bf16 v[68:71], v[176:179], v[44:47], v[68:71]\n"
    "v_mov_b32 v95, v92\n"
    "v_mov_b32 v96, v94\n"
    "v_mfma_f32_16x16x32_bf16 v[72:75], v[180:183], v[44:47], v[72:75]\n"
    "s_mov_b32 s10, 0x3727c5ac\n"
    "v_permlane32_swap_b32 v92, v95\n"
    "v_mfma_f32_16x16x32_bf16 v[76:79], a[24:27], v[44:47], v[76:79]\n"
    "buffer_load_dwordx4 v1, s[40:43], s7 offen offset:3072 sc1 lds\n"
    "ds_read_b128 v[44:47], v1 offset:12288\n"
    "v_mfma_f32_16x16x32_bf16 v[68:71], v[184:187], v[48:51], v[68:71]\n"
    "v_permlane32_swap_b32 v94, v96\n"
    "v_mul_f32 v98, s9, v92\n"
    "v_mfma_f32_16x16x32_bf16 v[72:75], v[188:191], v[48:51], v[72:75]\n"
    "v_mul_f32 v101, v98, v98\n"
    "v_fma_f32 v101, v95, s9, -v101\n"
    "v_mfma_f32_16x16x32_bf16 v[76:79], a[28:31], v[48:51], v[76:79]\n"
    "v_max_f32 v101, 0, v101\n"
    "ds_read_b128 v[48:51], v1 offset:13312\n"
    "s_waitcnt lgkmcnt(4)\n"
    "v_mfma_f32_16x16x32_bf16 v[68:71], v[192:195], v[52:55], v[68:71]\n"
    "v_add_f32 v101, s10, v101\n"
    "s_add_u32 m0, s8, 0xc000\n"
    "s_add_u32 s7, s22, 0xc000\n"
    "v_mfma_f32_16x16x32_bf16 v[72:75], v[196:199], v[52:55], v[72:75]\n"
    "v_sqrt_f32 v99, v101\n"
    "v_mfma_f32_16x16x32_bf16 v[76:79], a[32:35], v[52:55], v[76:79]\n"
    "v_rsq_f32 v100, v101\n"
    "ds_read_b128 v[52:55], v1 offset:14336\n"
    "v_mfma_f32_16x16x32_bf16 v[68:71], v[200:203], v[56:59], v[68:71]\n"
    "v_fma_f32 v94, -v98, s70, v94\n"
    "v_fma_f32 v94, v94, v100, s72\n"
    "v_mul_f32 v101, -2.0, v93\n"
    "v_mfma_f32_16x16x32_bf16 v[72:75], v[204:207], v[56:59], v[72:75]\n"
    "v_fma_f32 v101, v98, s68, v101\n"
    "v_fma_f32 v101, v98, v101, v96\n"
    "buffer_load_dwordx4 v1, s[40:43], s7 offen sc1 lds\n"
    "v_mfma_f32_16x16x32_bf16 v[76:79], a[36:39], v[56:59], v[76:79]\n"
    "v_fma_f32 v102, -v98, s69, v97\n"
    "ds_read_b128 v[56:59], v1 offset:15360\n"
    "s_waitcnt lgkmcnt(4)\n"
    "v_mfma_f32_16x16x32_bf16 v[68:71], v[208:211], v[60:63], v[68:71]\n"
    "v_add_f32 v102, v102, v102\n"
    "v_fma_f32 v101, v101, v100, v102\n"
    "v_mfma_f32_16x16x32_bf16 v[72:75], v[212:215], v[60:63], v[72:75]\n"
    "v_fma_f32 v101, v101, v100, s71\n"
    "v_mul_f32 v103, s9, v94\n"
    "v_mfma_f32_16x16x32_bf16 v[76:79], a[40:43], v[60:63], v[76:79]\n"
    "v_mul_f32 v102, v103, v103\n"
    "v_fma_f32 v101, v101, s9, -v102\n"
    "buffer_load_dwordx4 v1, s[40:43], s7 offen offset:1024 sc1 lds\n"
    "v_mfma_f32_16x16x32_bf16 v[68:71], v[216:219], v[64:67], v[68:71]\n"
    "v_max_f32 v101, 0, v101\n"
    "v_add_f32 v101, s10, v101\n"
    "v_mfma_f32_16x16x32_bf16 v[72:75], v[220:223], v[64:67], v[72:75]\n"
    "v_sqrt_f32 v104, v101\n"
    "v_mfma_f32_16x16x32_bf16 v[76:79], a[44:47], v[64:67], v[76:79]\n"
    "v_rsq_f32 v101, v101\n"
    "s_waitcnt lgkmcnt(2)\n"
    "v_mfma_f32_16x16x32_bf16 v[68:71], v[224:227], v[44:47], v[68:71]\n"
    "v_cmp_eq_u32 s[12:13], 1, v14\n"
    "v_mul_f32 v101, v100, v101\n"
    "v_mfma_f32_16x16x32_bf16 v[72:75], v[228:231], v[44:47], v[72:75]\n"
    "v_cmp_eq_u32 s[14:15], 2, v14\n"
    "v_mul_f32 v103, v103, v99\n"
    "v_mfma_f32_16x16x32_bf16 v[76:79], a[48:51], v[44:47], v[76:79]\n"
    "v_cmp_eq_u32 vcc, 3, v14\n"
    "v_mul_f32 v107, v104, v99\n"
    "buffer_load_dwordx4 v1, s[40:43], s7 offen offset:2048 sc1 lds\n"
    "v_mfma_f32_16x16x32_bf16 v[68:71], v[232:235], v[48:51], v[68:71]\n"
    "v_mul_f32 v105, 0xc0135761, v101\n"
    "v_mul_f32 v102, v101, v101\n"
    "v_mfma_f32_16x16x32_bf16 v[72:75], v[236:239], v[48:51], v[72:75]\n"
    "v_cndmask_b32 v104, -v98, v99, s[12:13]\n"
    "v_mul_f32 v102, 0x3d372713, v102\n"
    "v_mfma_f32_16x16x32_bf16 v[76:79], a[52:55], v[48:51], v[76:79]\n"
    "v_cndmask_b32 v104, v104, -v103, s[14:15]\n"
    "v_mul_f32 v106, v105, v102\n"
    "s_waitcnt lgkmcnt(0)\n"
    "v_mfma_f32_16x16x32_bf16 v[68:71], v[240:243], v[52:55], v[68:71]\n"
    "v_cndmask_b32 v104, v104, v107\n"
    "s_lshl_b32 s9, s6, 10\n"
    "v_mfma_f32_16x16x32_bf16 v[72:75], v[244:247], v[52:55], v[72:75]\n"
    "v_add_u32 v98, s9, v39\n"
    "ds_write_b128 v98, v[104:107]\n"
    "v_mfma_f32_16x16x32_bf16 v[76:79], a[56:59], v[52:55], v[76:79]\n"
    "v_mfma_f32_16x16x32_bf16 v[68:71], v[248:251], v[56:59], v[68:71]\n"
    "buffer_load_dwordx4 v1, s[40:43], s7 offen offset:3072 sc1 lds\n"
    "v_mfma_f32_16x16x32_bf16 v[72:75], v[252:255], v[56:59], v[72:75]\n"
    "v_mfma_f32_16x16x32_bf16 v[76:79], a[60:63], v[56:59], v[76:79]\n"
    "s_waitcnt vmcnt(8)\n"
    "s_waitcnt lgkmcnt(0)\n"
    "s_barrier\n"
    "ds_read_b128 v[108:111], v39\n"
    "ds_read_b128 v[60:63], v1 offset:16384\n"
    "ds_read_b128 v[64:67], v1 offset:17408\n"
    "ds_read_b128 v[44:47], v1 offset:18432\n"
    "ds_read_b128 v[48:51], v1 offset:19456\n"
    "ds_read_b128 v[52:55], v1 offset:20480\n"
    "ds_read_b128 v[56:59], v1 offset:21504\n"
    "s_waitcnt lgkmcnt(4)\n"
    "v_mfma_f32_16x16x32_bf16 v[80:83], v[128:131], v[60:63], 0\n"
    "s_add_u32 m0, s52, 0x18800\n"
    "v_mfma_f32_16x16x32_bf16 v[84:87], v[132:135], v[60:63], 0\n"
    "v_add_u32 v43, s19, v127\n"
    "v_mfma_f32_16x16x32_bf16 v[88:91], a[0:3], v[60:63], 0\n"
    "ds_read_b128 v[60:63], v1 offset:22528\n"
    "v_mfma_f32_16x16x32_bf16 v[80:83], v[136:139], v[64:67], v[80:83]\n"
    "buffer_load_dwordx4 v43, s[32:35], 0 offen lds\n"
    "v_mfma_f32_16x16x32_bf16 v[84:87], v[140:143], v[64:67], v[84:87]\n"
    "v_mfma_f32_16x16x32_bf16 v[88:91], a[4:7], v[64:67], v[88:91]\n"
    "v_mfma_f32_16x16x4_f32 v[68:71], v36, v108, v[68:71]\n"
    "buffer_load_dwordx4 v43, s[32:35], s89 offen offset:1088 lds\n"
    "v_mfma_f32_16x16x4_f32 v[72:75], v37, v108, v[72:75]\n"
    "v_mfma_f32_16x16x4_f32 v[76:79], v38, v108, v[76:79]\n"
    "ds_read_b128 v[64:67], v1 offset:23552\n"
    "s_waitcnt lgkmcnt(4)\n"
    "v_mfma_f32_16x16x32_bf16 v[80:83], v[144:147], v[44:47], v[80:83]\n"
    "buffer_load_dwordx4 v43, s[32:35], s90 offen offset:2176 lds\n"
    "v_mfma_f32_16x16x32_bf16 v[84:87], v[148:151], v[44:47], v[84:87]\n"
    "v_mfma_f32_16x16x32_bf16 v[88:91], a[8:11], v[44:47], v[88:91]\n"
    "ds_read_b128 v[44:47], v1 offset:24576\n"
    "v_mfma_f32_16x16x32_bf16 v[80:83], v[152:155], v[48:51], v[80:83]\n"
    "v_pk_mul_f32 v[92:93], v[68:69], v[68:69]\n"
    "v_pk_mul_f32 v[94:95], v[70:71], v[70:71]\n"
    "v_pk_mul_f32 v[96:97], v[72:73], v[72:73]\n"
    "v_pk_mul_f32 v[98:99], v[74:75], v[74:75]\n"
    "v_pk_mul_f32 v[100:101], v[76:77], v[76:77]\n"
    "v_pk_mul_f32 v[102:103], v[78:79], v[78:79]\n"
    "v_pk_fma_f32 v[92:93], v[92:93], v[110:111], v[108:109] op_sel:[0,0,1] op_sel_hi:[1,0,1]\n"
    "v_pk_fma_f32 v[94:95], v[94:95], v[110:111], v[108:109] op_sel:[0,0,1] op_sel_hi:[1,0,1]\n"
    "v_pk_fma_f32 v[96:97], v[96:97], v[110:111], v[108:109] op_sel:[0,0,1] op_sel_hi:[1,0,1]\n"
    "v_pk_fma_f32 v[98:99], v[98:99], v[110:111], v[108:109] op_sel:[0,0,1] op_sel_hi:[1,0,1]\n"
    "v_pk_fma_f32 v[100:101], v[100:101], v[110:111], v[108:109] op_sel:[0,0,1] op_sel_hi:[1,0,1]\n"
    "v_pk_fma_f32 v[102:103], v[102:103], v[110:111], v[108:109] op_sel:[0,0,1] op_sel_hi:[1,0,1]\n"
    "v_pk_mul_f32 v[92:93], v[92:93], v[68:69]\n"
    "v_pk_mul_f32 v[94:95], v[94:95], v[70:71]\n"
    "v_pk_mul_f32 v[96:97], v[96:97], v[72:73]\n"
    "v_pk_mul_f32 v[98:99], v[98:99], v[74:75]\n"
    "v_pk_mul_f32 v[100:101], v[100:101], v[76:77]\n"
    "v_pk_mul_f32 v[102:103], v[102:103], v[78:79]\n"
    "v_mfma_f32_16x16x32_bf16 v[84:87], v[156:159], v[48:51], v[84:87]\n"
    "v_exp_f32 v92, v92\n"
    "v_mfma_f32_16x16x32_bf16 v[88:91], a[12:15], v[48:51], v[88:91]\n"
    "buffer_load_dwordx4 v43, s[32:35], s91 offen offset:3264 lds\n"
    "ds_read_b128 v[48:51], v1 offset:25600\n"
    "s_waitcnt lgkmcnt(4)\n"
    "v_mfma_f32_16x16x32_bf16 v[80:83], v[160:163], v[52:55], v[80:83]\n"
    "v_exp_f32 v93, v93\n"
    "v_mfma_f32_16x16x32_bf16 v[84:87], v[164:167], v[52:55], v[84:87]\n"
    "v_exp_f32 v94, v94\n"
    "s_add_u32 m0, s52, 0x1cc00\n"
    "v_mfma_f32_16x16x32_bf16 v[88:91], a[16:19], v[52:55], v[88:91]\n"
    "v_exp_f32 v95, v95\n"
    "ds_read_b128 v[52:55], v1 offset:26624\n"
    "v_mfma_f32_16x16x32_bf16 v[80:83], v[168:171], v[56:59], v[80:83]\n"
    "v_exp_f32 v96, v96\n"
    "v_add_u32 v43, s24, v127\n"
    "v_mfma_f32_16x16x32_bf16 v[84:87], v[172:175], v[56:59], v[84:87]\n"
    "v_exp_f32 v97, v97\n"
    "buffer_load_dwordx4 v43, s[32:35], 0 offen lds\n"
    "v_mfma_f32_16x16x32_bf16 v[88:91], a[20:23], v[56:59], v[88:91]\n"
    "ds_read_b128 v[56:59], v1 offset:27648\n"
    "s_waitcnt lgkmcnt(4)\n"
    "v_mfma_f32_16x16x32_bf16 v[80:83], v[176:179], v[60:63], v[80:83]\n"
    "v_exp_f32 v98, v98\n"
    "buffer_load_dwordx4 v43, s[32:35], s89 offen offset:1088 lds\n"
    "v_mfma_f32_16x16x32_bf16 v[84:87], v[180:183], v[60:63], v[84:87]\n"
    "v_exp_f32 v99, v99\n"
    "v_mfma_f32_16x16x32_bf16 v[88:91], a[24:27], v[60:63], v[88:91]\n"
    "v_exp_f32 v100, v100\n"
    "ds_read_b128 v[60:63], v1 offset:28672\n"
    "v_mfma_f32_16x16x32_bf16 v[80:83], v[184:187], v[64:67], v[80:83]\n"
    "v_exp_f32 v101, v101\n"
    "v_mfma_f32_16x16x32_bf16 v[84:87], v[188:191], v[64:67], v[84:87]\n"
    "v_exp_f32 v102, v102\n"
    "buffer_load_dwordx4 v43, s[32:35], s90 offen offset:2176 lds\n"
    "v_mfma_f32_16x16x32_bf16 v[88:91], a[28:31], v[64:67], v[88:91]\n"
    "v_exp_f32 v103, v103\n"
    "ds_read_b128 v[64:67], v1 offset:29696\n"
    "s_waitcnt lgkmcnt(4)\n"
    "v_mfma_f32_16x16x32_bf16 v[80:83], v[192:195], v[44:47], v[80:83]\n"
    "v_pk_fma_f32 v[92:93], v[92:93], v[110:111], v[110:111] op_sel:[0,1,1] op_sel_hi:[1,1,1]\n"
    "v_pk_fma_f32 v[94:95], v[94:95], v[110:111], v[110:111] op_sel:[0,1,1] op_sel_hi:[1,1,1]\n"
    "v_pk_fma_f32 v[96:97], v[96:97], v[110:111], v[110:111] op_sel:[0,1,1] op_sel_hi:[1,1,1]\n"
    "v_pk_fma_f32 v[98:99], v[98:99], v[110:111], v[110:111] op_sel:[0,1,1] op_sel_hi:[1,1,1]\n"
    "v_pk_fma_f32 v[100:101], v[100:101], v[110:111], v[110:111] op_sel:[0,1,1] op_sel_hi:[1,1,1]\n"
    "v_pk_fma_f32 v[102:103], v[102:103], v[110:111], v[110:111] op_sel:[0,1,1] op_sel_hi:[1,1,1]\n"
    "v_mfma_f32_16x16x32_bf16 v[84:87], v[196:199], v[44:47], v[84:87]\n"
    "v_rcp_f32 v92, v92\n"
    "v_mfma_f32_16x16x32_bf16 v[88:91], a[32:35], v[44:47], v[88:91]\n"
    "v_rcp_f32 v93, v93\n"
    "ds_read_b128 v[44:47], v1 offset:30720\n"
    "v_mfma_f32_16x16x32_bf16 v[80:83], v[200:203], v[48:51], v[80:83]\n"
    "v_rcp_f32 v94, v94\n"
    "v_mfma_f32_16x16x32_bf16 v[84:87], v[204:207], v[48:51], v[84:87]\n"
    "v_rcp_f32 v95, v95\n"
    "buffer_load_dwordx4 v43, s[32:35], s91 offen offset:3264 lds\n"
    "v_mfma_f32_16x16x32_bf16 v[88:91], a[36:39], v[48:51], v[88:91]\n"
    "ds_read_b128 v[48:51], v1 offset:31744\n"
    "s_waitcnt lgkmcnt(4)\n"
    "v_mfma_f32_16x16x32_bf16 v[80:83], v[208:211], v[52:55], v[80:83]\n"
    "v_rcp_f32 v96, v96\n"
    "s_add_u32 m0, s52, 0x21000\n"
    "v_mfma_f32_16x16x32_bf16 v[84:87], v[212:215], v[52:55], v[84:87]\n"
    "v_rcp_f32 v97, v97\n"
    "v_add_u32 v43, s25, v127\n"
    "v_mfma_f32_16x16x32_bf16 v[88:91], a[40:43], v[52:55], v[88:91]\n"
    "s_waitcnt vmcnt(8)\n"
    "s_barrier\n"
    "ds_read_b128 v[52:55], v1 offset:32768\n"
    "v_mfma_f32_16x16x32_bf16 v[80:83], v[216:219], v[56:59], v[80:83]\n"
    "v_rcp_f32 v98, v98\n"
    "buffer_load_dwordx4 v43, s[32:35], 0 offen lds\n"
    "v_mfma_f32_16x16x32_bf16 v[84:87], v[220:223], v[56:59], v[84:87]\n"
    "v_rcp_f32 v99, v99\n"
    "v_mfma_f32_16x16x32_bf16 v[88:91], a[44:47], v[56:59], v[88:91]\n"
    "v_rcp_f32 v100, v100\n"
    "ds_read_b128 v[56:59], v1 offset:33792\n"
    "s_waitcnt lgkmcnt(4)\n"
    "v_mfma_f32_16x16x32_bf16 v[80:83], v[224:227], v[60:63], v[80:83]\n"
    "v_rcp_f32 v101, v101\n"
    "buffer_load_dwordx4 v43, s[32:35], s89 offen offset:1088 lds\n"
    "v_mfma_f32_16x16x32_bf16 v[84:87], v[228:231], v[60:63], v[84:87]\n"
    "v_rcp_f32 v102, v102\n"
    "v_mfma_f32_16x16x32_bf16 v[88:91], a[48:51], v[60:63], v[88:91]\n"
    "v_rcp_f32 v103, v103\n"
    "ds_read_b128 v[60:63], v1 offset:34816\n"
    "v_mfma_f32_16x16x32_bf16 v[80:83], v[232:235], v[64:67], v[80:83]\n"
    "v_pk_mul_f32 v[68:69], v[68:69], v[92:93]\n"
    "v_pk_mul_f32 v[70:71], v[70:71], v[94:95]\n"
    "v_pk_mul_f32 v[72:73], v[72:73], v[96:97]\n"
    "v_pk_mul_f32 v[74:75], v[74:75], v[98:99]\n"
    "v_pk_mul_f32 v[76:77], v[76:77], v[100:101]\n"
    "v_pk_mul_f32 v[78:79], v[78:79], v[102:103]\n"
    "v_mfma_f32_16x16x32_bf16 v[84:87], v[236:239], v[64:67], v[84:87]\n"
    "s_add_u32 s21, s0, 0\n"
    "v_cvt_pk_bf16_f32 v104, v68, v69\n"
    "buffer_load_dwordx4 v43, s[32:35], s90 offen offset:2176 lds\n"
    "v_mfma_f32_16x16x32_bf16 v[88:91], a[52:55], v[64:67], v[88:91]\n"
    "v_cvt_pk_bf16_f32 v105, v70, v71\n"
    "ds_read_b128 v[64:67], v1 offset:35840\n"
    "s_waitcnt lgkmcnt(4)\n"
    "v_mfma_f32_16x16x32_bf16 v[80:83], v[240:243], v[44:47], v[80:83]\n"
    "v_cvt_pk_bf16_f32 v106, v72, v73\n"
    "v_cvt_pk_bf16_f32 v107, v74, v75\n"
    "v_mfma_f32_16x16x32_bf16 v[84:87], v[244:247], v[44:47], v[84:87]\n"
    "v_min_u32 v104, -2, v104\n"
    "v_min_u32 v105, -2, v105\n"
    "v_mfma_f32_16x16x32_bf16 v[88:91], a[56:59], v[44:47], v[88:91]\n"
    "v_min_u32 v106, -2, v106\n"
    "buffer_load_dwordx4 v43, s[32:35], s91 offen offset:3264 lds\n"
    "ds_read_b128 v[44:47], v1 offset:36864\n"
    "v_mfma_f32_16x16x32_bf16 v[80:83], v[248:251], v[48:51], v[80:83]\n"
    "v_min_u32 v107, -2, v107\n"
    "buffer_store_dwordx4 v[104:107], v1, s[40:43], s21 offen sc1\n"
    "v_mfma_f32_16x16x32_bf16 v[84:87], v[252:255], v[48:51], v[84:87]\n"
    "v_cvt_pk_bf16_f32 v112, v76, v77\n"
    "v_cvt_pk_bf16_f32 v113, v78, v79\n"
    "v_mfma_f32_16x16x32_bf16 v[88:91], a[60:63], v[48:51], v[88:91]\n"
    "v_min_u32 v112, -2, v112\n"
    "v_min_u32 v113, -2, v113\n"
    "ds_read_b128 v[108:111], v39 offset:1024\n"
    "ds_read_b128 v[48:51], v1 offset:37888\n"
    "s_waitcnt lgkmcnt(5)\n"
    "v_mfma_f32_16x16x32_bf16 v[68:71], v[128:131], v[52:55], 0\n"
    "s_add_u32 m0, s52, 0x10000\n"
    "v_mfma_f32_16x16x32_bf16 v[72:75], v[132:135], v[52:55], 0\n"
    "v_add_u32 v43, s26, v127\n"
    "v_mfma_f32_16x16x32_bf16 v[76:79], a[0:3], v[52:55], 0\n"
    "ds_read_b128 v[52:55], v1 offset:38912\n"
    "v_mfma_f32_16x16x32_bf16 v[68:71], v[136:139], v[56:59], v[68:71]\n"
    "buffer_load_dwordx4 v43, s[32:35], 0 offen lds\n"
    "v_mfma_f32_16x16x32_bf16 v[72:75], v[140:143], v[56:59], v[72:75]\n"
    "v_mfma_f32_16x16x32_bf16 v[76:79], a[4:7], v[56:59], v[76:79]\n"
    "s_waitcnt lgkmcnt(2)\n"
    "v_mfma_f32_16x16x4_f32 v[80:83], v36, v108, v[80:83]\n"
    "buffer_load_dwordx4 v43, s[32:35], s89 offen offset:1088 lds\n"
    "v_mfma_f32_16x16x4_f32 v[84:87], v37, v108, v[84:87]\n"
    "v_mfma_f32_16x16x4_f32 v[88:91], v38, v108, v[88:91]\n"
    "ds_read_b128 v[56:59], v1 offset:39936\n"
    "v_mfma_f32_16x16x32_bf16 v[68:71], v[144:147], v[60:63], v[68:71]\n"
    "buffer_load_dwordx4 v43, s[32:35], s90 offen offset:2176 lds\n"
    "v_mfma_f32_16x16x32_bf16 v[72:75], v[148:151], v[60:63], v[72:75]\n"
    "v_mfma_f32_16x16x32_bf16 v[76:79], a[8:11], v[60:63], v[76:79]\n"
    "ds_read_b128 v[60:63], v1 offset:40960\n"
    "v_mfma_f32_16x16x32_bf16 v[68:71], v[152:155], v[64:67], v[68:71]\n"
    "v_pk_mul_f32 v[92:93], v[80:81], v[80:81]\n"
    "v_pk_mul_f32 v[94:95], v[82:83], v[82:83]\n"
    "v_pk_mul_f32 v[96:97], v[84:85], v[84:85]\n"
    "v_pk_mul_f32 v[98:99], v[86:87], v[86:87]\n"
    "v_pk_mul_f32 v[100:101], v[88:89], v[88:89]\n"
    "v_pk_mul_f32 v[102:103], v[90:91], v[90:91]\n"
    "v_pk_fma_f32 v[92:93], v[92:93], v[110:111], v[108:109] op_sel:[0,0,1] op_sel_hi:[1,0,1]\n"
    "v_pk_fma_f32 v[94:95], v[94:95], v[110:111], v[108:109] op_sel:[0,0,1] op_sel_hi:[1,0,1]\n"
    "v_pk_fma_f32 v[96:97], v[96:97], v[110:111], v[108:109] op_sel:[0,0,1] op_sel_hi:[1,0,1]\n"
    "v_pk_fma_f32 v[98:99], v[98:99], v[110:111], v[108:109] op_sel:[0,0,1] op_sel_hi:[1,0,1]\n"
    "v_pk_fma_f32 v[100:101], v[100:101], v[110:111], v[108:109] op_sel:[0,0,1] op_sel_hi:[1,0,1]\n"
    "v_pk_fma_f32 v[102:103], v[102:103], v[110:111], v[108:109] op_sel:[0,0,1] op_sel_hi:[1,0,1]\n"
    "v_pk_mul_f32 v[92:93], v[92:93], v[80:81]\n"
    "v_pk_mul_f32 v[94:95], v[94:95], v[82:83]\n"
    "v_pk_mul_f32 v[96:97], v[96:97], v[84:85]\n"
    "v_pk_mul_f32 v[98:99], v[98:99], v[86:87]\n"
    "v_pk_mul_f32 v[100:101], v[100:101], v[88:89]\n"
    "v_pk_mul_f32 v[102:103], v[102:103], v[90:91]\n"
    "v_mfma_f32_16x16x32_bf16 v[72:75], v[156:159], v[64:67], v[72:75]\n"
    "v_exp_f32 v92, v92\n"
    "v_mfma_f32_16x16x32_bf16 v[76:79], a[12:15], v[64:67], v[76:79]\n"
    "buffer_load_dwordx4 v43, s[32:35], s91 offen offset:3264 lds\n"
    "ds_read_b128 v[64:67], v1 offset:41984\n"
    "s_waitcnt lgkmcnt(4)\n"
    "v_mfma_f32_16x16x32_bf16 v[68:71], v[160:163], v[44:47], v[68:71]\n"
    "v_exp_f32 v93, v93\n"
    "v_mfma_f32_16x16x32_bf16 v[72:75], v[164:167], v[44:47], v[72:75]\n"
    "v_exp_f32 v94, v94\n"
    "s_add_u32 m0, s52, 0x14400\n"
    "v_mfma_f32_16x16x32_bf16 v[76:79], a[16:19], v[44:47], v[76:79]\n"
    "v_exp_f32 v95, v95\n"
    "ds_read_b128 v[44:47], v1 offset:43008\n"
    "v_mfma_f32_16x16x32_bf16 v[68:71], v[168:171], v[48:51], v[68:71]\n"
    "v_exp_f32 v96, v96\n"
    "v_add_u32 v43, s27, v127\n"
    "v_mfma_f32_16x16x32_bf16 v[72:75], v[172:175], v[48:51], v[72:75]\n"
    "v_exp_f32 v97, v97\n"
    "buffer_load_dwordx4 v43, s[32:35], 0 offen lds\n"
    "v_mfma_f32_16x16x32_bf16 v[76:79], a[20:23], v[48:51], v[76:79]\n"
    "ds_read_b128 v[48:51], v1 offset:44032\n"
    "s_waitcnt lgkmcnt(4)\n"
    "v_mfma_f32_16x16x32_bf16 v[68:71], v[176:179], v[52:55], v[68:71]\n"
    "v_exp_f32 v98, v98\n"
    "buffer_load_dwordx4 v43, s[32:35], s89 offen offset:1088 lds\n"
    "v_mfma_f32_16x16x32_bf16 v[72:75], v[180:183], v[52:55], v[72:75]\n"
    "v_exp_f32 v99, v99\n"
    "v_mfma_f32_16x16x32_bf16 v[76:79], a[24:27], v[52:55], v[76:79]\n"
    "v_exp_f32 v100, v100\n"
    "ds_read_b128 v[52:55], v1 offset:45056\n"
    "v_mfma_f32_16x16x32_bf16 v[68:71], v[184:187], v[56:59], v[68:71]\n"
    "v_exp_f32 v101, v101\n"
    "v_mfma_f32_16x16x32_bf16 v[72:75], v[188:191], v[56:59], v[72:75]\n"
    "v_exp_f32 v102, v102\n"
    "buffer_load_dwordx4 v43, s[32:35], s90 offen offset:2176 lds\n"
    "v_mfma_f32_16x16x32_bf16 v[76:79], a[28:31], v[56:59], v[76:79]\n"
    "v_exp_f32 v103, v103\n"
    "ds_read_b128 v[56:59], v1 offset:46080\n"
    "s_waitcnt lgkmcnt(4)\n"
    "v_mfma_f32_16x16x32_bf16 v[68:71], v[192:195], v[60:63], v[68:71]\n"
    "v_pk_fma_f32 v[92:93], v[92:93], v[110:111], v[110:111] op_sel:[0,1,1] op_sel_hi:[1,1,1]\n"
    "v_pk_fma_f32 v[94:95], v[94:95], v[110:111], v[110:111] op_sel:[0,1,1] op_sel_hi:[1,1,1]\n"
    "v_pk_fma_f32 v[96:97], v[96:97], v[110:111], v[110:111] op_sel:[0,1,1] op_sel_hi:[1,1,1]\n"
    "v_pk_fma_f32 v[98:99], v[98:99], v[110:111], v[110:111] op_sel:[0,1,1] op_sel_hi:[1,1,1]\n"
    "v_pk_fma_f32 v[100:101], v[100:101], v[110:111], v[110:111] op_sel:[0,1,1] op_sel_hi:[1,1,1]\n"
    "v_pk_fma_f32 v[102:103], v[102:103], v[110:111], v[110:111] op_sel:[0,1,1] op_sel_hi:[1,1,1]\n"
    "v_mfma_f32_16x16x32_bf16 v[72:75], v[196:199], v[60:63], v[72:75]\n"
    "v_rcp_f32 v92, v92\n"
    "v_mfma_f32_16x16x32_bf16 v[76:79], a[32:35], v[60:63], v[76:79]\n"
    "buffer_load_dwordx4 v43, s[32:35], s91 offen offset:3264 lds\n"
    "ds_read_b128 v[60:63], v1 offset:47104\n"
    "v_mfma_f32_16x16x32_bf16 v[68:71], v[200:203], v[64:67], v[68:71]\n"
    "v_rcp_f32 v93, v93\n"
    "v_mfma_f32_16x16x32_bf16 v[72:75], v[204:207], v[64:67], v[72:75]\n"
    "v_rcp_f32 v94, v94\n"
    "v_mfma_f32_16x16x32_bf16 v[76:79], a[36:39], v[64:67], v[76:79]\n"
    "v_rcp_f32 v95, v95\n"
    "ds_read_b128 v[64:67], v1 offset:48128\n"
    "s_waitcnt lgkmcnt(4)\n"
    "v_mfma_f32_16x16x32_bf16 v[68:71], v[208:211], v[44:47], v[68:71]\n"
    "v_rcp_f32 v96, v96\n"
    "s_waitcnt vmcnt(17)\n"
    "ds_read_b64_tr_b16 a[112:113], v6 offset:34816\n"
    "ds_read_b64_tr_b16 a[114:115], v6 offset:36992\n"
    "v_mfma_f32_16x16x32_bf16 v[72:75], v[212:215], v[44:47], v[72:75]\n"
    "v_rcp_f32 v97, v97\n"
    "ds_read_b64_tr_b16 a[116:117], v7 offset:34816\n"
    "ds_read_b64_tr_b16 a[118:119], v7 offset:36992\n"
    "ds_read_b64_tr_b16 a[120:121], v8 offset:34816\n"
    "v_mfma_f32_16x16x32_bf16 v[76:79], a[40:43], v[44:47], v[76:79]\n"
    "v_rcp_f32 v98, v98\n"
    "ds_read_b64_tr_b16 a[122:123], v8 offset:36992\n"
    "ds_read_b64_tr_b16 a[124:125], v9 offset:34816\n"
    "ds_read_b64_tr_b16 a[126:127], v9 offset:36992\n"
    "s_waitcnt lgkmcnt(0)\n"
    "ds_read_b128 v[44:47], v1 offset:49152\n"
    "v_mfma_f32_16x16x32_bf16 v[68:71], v[216:219], v[48:51], v[68:71]\n"
    "v_rcp_f32 v99, v99\n"
    "s_add_u32 m0, s52, 0x18800\n"
    "v_add_u32 v43, s37, v127\n"
    "buffer_load_dwordx4 v43, s[32:35], 0 offen lds\n"
    "v_mfma_f32_16x16x32_bf16 v[72:75], v[220:223], v[48:51], v[72:75]\n"
    "v_rcp_f32 v100, v100\n"
    "v_mfma_f32_16x16x32_bf16 v[76:79], a[44:47], v[48:51], v[76:79]\n"
    "buffer_load_dwordx4 v43, s[32:35], s89 offen offset:1088 lds\n"
    "ds_read_b128 v[48:51], v1 offset:50176\n"
    "v_mfma_f32_16x16x32_bf16 v[68:71], v[224:227], v[52:55], v[68:71]\n"
    "v_rcp_f32 v101, v101\n"
    "v_mfma_f32_16x16x32_bf16 v[72:75], v[228:231], v[52:55], v[72:75]\n"
    "v_rcp_f32 v102, v102\n"
    "v_mfma_f32_16x16x32_bf16 v[76:79], a[48:51], v[52:55], v[76:79]\n"
    "v_rcp_f32 v103, v103\n"
    "ds_read_b128 v[52:55], v1 offset:51200\n"
    "v_mfma_f32_16x16x32_bf16 v[68:71], v[232:235], v[56:59], v[68:71]\n"
    "v_pk_mul_f32 v[80:81], v[80:81], v[92:93]\n"
    "v_pk_mul_f32 v[82:83], v[82:83], v[94:95]\n"
    "v_pk_mul_f32 v[84:85], v[84:85], v[96:97]\n"
    "v_pk_mul_f32 v[86:87], v[86:87], v[98:99]\n"
    "v_pk_mul_f32 v[88:89], v[88:89], v[100:101]\n"
    "v_pk_mul_f32 v[90:91], v[90:91], v[102:103]\n"
    "v_mfma_f32_16x16x32_bf16 v[72:75], v[236:239], v[56:59], v[72:75]\n"
    "s_add_u32 s21, s0, 0xb000\n"
    "v_cvt_pk_bf16_f32 v104, v80, v81\n"
    "v_cvt_pk_bf16_f32 v105, v82, v83\n"
    "buffer_load_dwordx4 v43, s[32:35], s90 offen offset:2176 lds\n"
    "v_mfma_f32_16x16x32_bf16 v[76:79], a[52:55], v[56:59], v[76:79]\n"
    "v_cvt_pk_bf16_f32 v106, v84, v85\n"
    "ds_read_b128 v[56:59], v1 offset:52224\n"
    "v_mfma_f32_16x16x32_bf16 v[68:71], v[240:243], v[60:63], v[68:71]\n"
    "v_cvt_pk_bf16_f32 v107, v86, v87\n"
    "v_min_u32 v104, -2, v104\n"
    "buffer_load_dwordx4 v43, s[32:35], s91 offen offset:3264 lds\n"
    "v_mfma_f32_16x16x32_bf16 v[72:75], v[244:247], v[60:63], v[72:75]\n"
    "v_min_u32 v105, -2, v105\n"
    "v_min_u32 v106, -2, v106\n"
    "v_min_u32 v107, -2, v107\n"
    "v_mfma_f32_16x16x32_bf16 v[76:79], a[56:59], v[60:63], v[76:79]\n"
    "buffer_store_dwordx4 v[104:107], v1, s[40:43], s21 offen sc1\n"
    "v_cvt_pk_bf16_f32 v114, v88, v89\n"
    "ds_read_b128 v[60:63], v1 offset:53248\n"
    "v_mfma_f32_16x16x32_bf16 v[68:71], v[248:251], v[64:67], v[68:71]\n"
    "v_cvt_pk_bf16_f32 v115, v90, v91\n"
    "v_min_u32 v114, -2, v114\n"
    "v_min_u32 v115, -2, v115\n"
    "v_mfma_f32_16x16x32_bf16 v[72:75], v[252:255], v[64:67], v[72:75]\n"
    "v_permlane16_swap_b32 v112, v114\n"
    "v_mfma_f32_16x16x32_bf16 v[76:79], a[60:63], v[64:67], v[76:79]\n"
    "v_permlane16_swap_b32 v113, v115\n"
    "buffer_store_dwordx4 v[112:115], v126, s[40:43], s1 offen sc1\n"
    "ds_read_b128 v[108:111], v39 offset:2048\n"
    "ds_read_b128 v[64:67], v1 offset:54272\n"
    "s_waitcnt lgkmcnt(5)\n"
    "v_mfma_f32_16x16x32_bf16 v[80:83], v[128:131], v[44:47], 0\n"
    "s_waitcnt vmcnt(19)\n"
    "ds_read_b64_tr_b16 a[128:129], v6 offset:52224\n"
    "v_mfma_f32_16x16x32_bf16 v[84:87], v[132:135], v[44:47], 0\n"
    "ds_read_b64_tr_b16 a[130:131], v6 offset:54400\n"
    "ds_read_b64_tr_b16 a[132:133], v7 offset:52224\n"
    "v_mfma_f32_16x16x32_bf16 v[88:91], a[0:3], v[44:47], 0\n"
    "ds_read_b64_tr_b16 a[134:135], v7 offset:54400\n"
    "ds_read_b64_tr_b16 a[136:137], v8 offset:52224\n"
    "ds_read_b128 v[44:47], v1 offset:55296\n"
    "v_mfma_f32_16x16x32_bf16 v[80:83], v[136:139], v[48:51], v[80:83]\n"
    "ds_read_b64_tr_b16 a[138:139], v8 offset:54400\n"
    "ds_read_b64_tr_b16 a[140:141], v9 offset:52224\n"
    "v_mfma_f32_16x16x32_bf16 v[84:87], v[140:143], v[48:51], v[84:87]\n"
    "ds_read_b64_tr_b16 a[142:143], v9 offset:54400\n"
    "s_waitcnt lgkmcnt(0)\n"
    "s_add_u32 m0, s52, 0x1cc00\n"
    "v_mfma_f32_16x16x32_bf16 v[88:91], a[4:7], v[48:51], v[88:91]\n"
    "v_add_u32 v43, s56, v127\n"
    "buffer_load_dwordx4 v43, s[32:35], 0 offen lds\n"
    "v_mfma_f32_16x16x4_f32 v[68:71], v36, v108, v[68:71]\n"
    "v_mfma_f32_16x16x4_f32 v[72:75], v37, v108, v[72:75]\n"
    "buffer_load_dwordx4 v43, s[32:35], s89 offen offset:1088 lds\n"
    "v_mfma_f32_16x16x4_f32 v[76:79], v38, v108, v[76:79]\n"
    "ds_read_b128 v[48:51], v1 offset:56320\n"
    "v_mfma_f32_16x16x32_bf16 v[80:83], v[144:147], v[52:55], v[80:83]\n"
    "v_mfma_f32_16x16x32_bf16 v[84:87], v[148:151], v[52:55], v[84:87]\n"
    "v_mfma_f32_16x16x32_bf16 v[88:91], a[8:11], v[52:55], v[88:91]\n"
    "ds_read_b128 v[52:55], v1 offset:57344\n"
    "v_mfma_f32_16x16x32_bf16 v[80:83], v[152:155], v[56:59], v[80:83]\n"
    "s_nop 0\n"
    "v_pk_mul_f32 v[92:93], v[68:69], v[68:69]\n"
    "v_pk_mul_f32 v[94:95], v[70:71], v[70:71]\n"
    "v_pk_mul_f32 v[96:97], v[72:73], v[72:73]\n"
    "v_pk_mul_f32 v[98:99], v[74:75], v[74:75]\n"
    "v_pk_mul_f32 v[100:101], v[76:77], v[76:77]\n"
    "v_pk_mul_f32 v[102:103], v[78:79], v[78:79]\n"
    "v_pk_fma_f32 v[92:93], v[92:93], v[110:111], v[108:109] op_sel:[0,0,1] op_sel_hi:[1,0,1]\n"
    "v_pk_fma_f32 v[94:95], v[94:95], v[110:111], v[108:109] op_sel:[0,0,1] op_sel_hi:[1,0,1]\n"
    "v_pk_fma_f32 v[96:97], v[96:97], v[110:111], v[108:109] op_sel:[0,0,1] op_sel_hi:[1,0,1]\n"
    "v_pk_fma_f32 v[98:99], v[98:99], v[110:111], v[108:109] op_sel:[0,0,1] op_sel_hi:[1,0,1]\n"
    "v_pk_fma_f32 v[100:101], v[100:101], v[110:111], v[108:109] op_sel:[0,0,1] op_sel_hi:[1,0,1]\n"
    "v_pk_fma_f32 v[102:103], v[102:103], v[110:111], v[108:109] op_sel:[0,0,1] op_sel_hi:[1,0,1]\n"
    "v_pk_mul_f32 v[92:93], v[92:93], v[68:69]\n"
    "v_pk_mul_f32 v[94:95], v[94:95], v[70:71]\n"
    "v_pk_mul_f32 v[96:97], v[96:97], v[72:73]\n"
    "v_pk_mul_f32 v[98:99], v[98:99], v[74:75]\n"
    "v_pk_mul_f32 v[100:101], v[100:101], v[76:77]\n"
    "v_pk_mul_f32 v[102:103], v[102:103], v[78:79]\n"
    "v_mfma_f32_16x16x32_bf16 v[84:87], v[156:159], v[56:59], v[84:87]\n"
    "v_exp_f32 v92, v92\n"
    "v_mfma_f32_16x16x32_bf16 v[88:91], a[12:15], v[56:59], v[88:91]\n"
    "buffer_load_dwordx4 v43, s[32:35], s90 offen offset:2176 lds\n"
    "ds_read_b128 v[56:59], v1 offset:58368\n"
    "v_mfma_f32_16x16x32_bf16 v[80:83], v[160:163], v[60:63], v[80:83]\n"
    "v_exp_f32 v93, v93\n"
    "v_mfma_f32_16x16x32_bf16 v[84:87], v[164:167], v[60:63], v[84:87]\n"
    "v_exp_f32 v94, v94\n"
    "v_mfma_f32_16x16x32_bf16 v[88:91], a[16:19], v[60:63], v[88:91]\n"
    "v_exp_f32 v95, v95\n"
    "ds_read_b128 v[60:63], v1 offset:59392\n"
    "v_mfma_f32_16x16x32_bf16 v[80:83], v[168:171], v[64:67], v[80:83]\n"
    "v_exp_f32 v96, v96\n"
    "v_mfma_f32_16x16x32_bf16 v[84:87], v[172:175], v[64:67], v[84:87]\n"
    "v_exp_f32 v97, v97\n"
    "v_mfma_f32_16x16x32_bf16 v[88:91], a[20:23], v[64:67], v[88:91]\n"
    "buffer_load_dwordx4 v43, s[32:35], s91 offen offset:3264 lds\n"
    "ds_read_b128 v[64:67], v1 offset:60416\n"
    "s_waitcnt lgkmcnt(4)\n"
    "v_mfma_f32_16x16x32_bf16 v[80:83], v[176:179], v[44:47], v[80:83]\n"
    "v_exp_f32 v98, v98\n"
    "v_mfma_f32_16x16x32_bf16 v[84:87], v[180:183], v[44:47], v[84:87]\n"
    "v_exp_f32 v99, v99\n"
    "v_mfma_f32_16x16x32_bf16 v[88:91], a[24:27], v[44:47], v[88:91]\n"
    "v_exp_f32 v100, v100\n"
    "ds_read_b128 v[44:47], v1 offset:61440\n"
    "v_mfma_f32_16x16x32_bf16 v[80:83], v[184:187], v[48:51], v[80:83]\n"
    "v_exp_f32 v101, v101\n"
    "v_mfma_f32_16x16x32_bf16 v[84:87], v[188:191], v[48:51], v[84:87]\n"
    "v_exp_f32 v102, v102\n"
    "s_waitcnt vmcnt(19)\n"
    "v_add_u32 v6, 0xcc00, v6\n"
    "v_add_u32 v7, 0xcc00, v7\n"
    "v_mfma_f32_16x16x32_bf16 v[88:91], a[28:31], v[48:51], v[88:91]\n"
    "v_exp_f32 v103, v103\n"
    "v_add_u32 v8, 0xcc00, v8\n"
    "v_add_u32 v9, 0xcc00, v9\n"
    "ds_read_b128 v[48:51], v1 offset:62464\n"
    "s_waitcnt lgkmcnt(4)\n"
    "v_mfma_f32_16x16x32_bf16 v[80:83], v[192:195], v[52:55], v[80:83]\n"
    "v_pk_fma_f32 v[92:93], v[92:93], v[110:111], v[110:111] op_sel:[0,1,1] op_sel_hi:[1,1,1]\n"
    "v_pk_fma_f32 v[94:95], v[94:95], v[110:111], v[110:111] op_sel:[0,1,1] op_sel_hi:[1,1,1]\n"
    "v_pk_fma_f32 v[96:97], v[96:97], v[110:111], v[110:111] op_sel:[0,1,1] op_sel_hi:[1,1,1]\n"
    "v_pk_fma_f32 v[98:99], v[98:99], v[110:111], v[110:111] op_sel:[0,1,1] op_sel_hi:[1,1,1]\n"
    "v_pk_fma_f32 v[100:101], v[100:101], v[110:111], v[110:111] op_sel:[0,1,1] op_sel_hi:[1,1,1]\n"
    "v_pk_fma_f32 v[102:103], v[102:103], v[110:111], v[110:111] op_sel:[0,1,1] op_sel_hi:[1,1,1]\n"
    "v_mfma_f32_16x16x32_bf16 v[84:87], v[196:199], v[52:55], v[84:87]\n"
    "v_rcp_f32 v92, v92\n"
    "ds_read_b64_tr_b16 a[144:145], v6 offset:17408\n"
    "ds_read_b64_tr_b16 a[146:147], v6 offset:19584\n"
    "ds_read_b64_tr_b16 a[148:149], v7 offset:17408\n"
    "v_mfma_f32_16x16x32_bf16 v[88:91], a[32:35], v[52:55], v[88:91]\n"
    "ds_read_b64_tr_b16 a[150:151], v7 offset:19584\n"
    "ds_read_b64_tr_b16 a[152:153], v8 offset:17408\n"
    "ds_read_b64_tr_b16 a[154:155], v8 offset:19584\n"
    "ds_read_b64_tr_b16 a[156:157], v9 offset:17408\n"
    "ds_read_b64_tr_b16 a[158:159], v9 offset:19584\n"
    "s_waitcnt lgkmcnt(0)\n"
    "ds_read_b128 v[52:55], v1 offset:63488\n"
    "v_mfma_f32_16x16x32_bf16 v[80:83], v[200:203], v[56:59], v[80:83]\n"
    "v_rcp_f32 v93, v93\n"
    "s_add_u32 m0, s52, 0x21000\n"
    "v_add_u32 v43, s57, v127\n"
    "buffer_load_dwordx4 v43, s[32:35], 0 offen lds\n"
    "v_mfma_f32_16x16x32_bf16 v[84:87], v[204:207], v[56:59], v[84:87]\n"
    "v_rcp_f32 v94, v94\n"
    "v_mfma_f32_16x16x32_bf16 v[88:91], a[36:39], v[56:59], v[88:91]\n"
    "ds_read_b128 v[56:59], v1 offset:64512\n"
    "v_mfma_f32_16x16x32_bf16 v[80:83], v[208:211], v[60:63], v[80:83]\n"
    "v_rcp_f32 v95, v95\n"
    "buffer_load_dwordx4 v43, s[32:35], s89 offen offset:1088 lds\n"
    "v_mfma_f32_16x16x32_bf16 v[84:87], v[212:215], v[60:63], v[84:87]\n"
    "v_rcp_f32 v96, v96\n"
    "v_mfma_f32_16x16x32_bf16 v[88:91], a[40:43], v[60:63], v[88:91]\n"
    "v_rcp_f32 v97, v97\n"
    "v_mfma_f32_16x16x32_bf16 v[80:83], v[216:219], v[64:67], v[80:83]\n"
    "v_rcp_f32 v98, v98\n"
    "v_mfma_f32_16x16x32_bf16 v[84:87], v[220:223], v[64:67], v[84:87]\n"
    "v_rcp_f32 v99, v99\n"
    "v_mfma_f32_16x16x32_bf16 v[88:91], a[44:47], v[64:67], v[88:91]\n"
    "v_rcp_f32 v100, v100\n"
    "v_mfma_f32_16x16x32_bf16 v[80:83], v[224:227], v[44:47], v[80:83]\n"
    "v_rcp_f32 v101, v101\n"
    "buffer_load_dwordx4 v43, s[32:35], s90 offen offset:2176 lds\n"
    "v_mfma_f32_16x16x32_bf16 v[84:87], v[228:231], v[44:47], v[84:87]\n"
    "v_rcp_f32 v102, v102\n"
    "v_mfma_f32_16x16x32_bf16 v[88:91], a[48:51], v[44:47], v[88:91]\n"
    "v_rcp_f32 v103, v103\n"
    "v_mfma_f32_16x16x32_bf16 v[80:83], v[232:235], v[48:51], v[80:83]\n"
    "v_pk_mul_f32 v[68:69], v[68:69], v[92:93]\n"
    "v_pk_mul_f32 v[70:71], v[70:71], v[94:95]\n"
    "v_pk_mul_f32 v[72:73], v[72:73], v[96:97]\n"
    "v_pk_mul_f32 v[74:75], v[74:75], v[98:99]\n"
    "v_pk_mul_f32 v[76:77], v[76:77], v[100:101]\n"
    "v_pk_mul_f32 v[78:79], v[78:79], v[102:103]\n"
    "v_mfma_f32_16x16x32_bf16 v[84:87], v[236:239], v[48:51], v[84:87]\n"
    "s_add_u32 s21, s0, 0x16000\n"
    "v_cvt_pk_bf16_f32 v104, v68, v69\n"
    "v_mfma_f32_16x16x32_bf16 v[88:91], a[52:55], v[48:51], v[88:91]\n"
    "v_cvt_pk_bf16_f32 v105, v70, v71\n"
    "s_waitcnt lgkmcnt(0)\n"
    "v_mfma_f32_16x16x32_bf16 v[80:83], v[240:243], v[52:55], v[80:83]\n"
    "v_cvt_pk_bf16_f32 v106, v72, v73\n"
    "v_cvt_pk_bf16_f32 v107, v74, v75\n"
    "v_mfma_f32_16x16x32_bf16 v[84:87], v[244:247], v[52:55], v[84:87]\n"
    "v_min_u32 v104, -2, v104\n"
    "v_min_u32 v105, -2, v105\n"
    "v_mfma_f32_16x16x32_bf16 v[88:91], a[56:59], v[52:55], v[88:91]\n"
    "v_min_u32 v106, -2, v106\n"
    "buffer_load_dwordx4 v43, s[32:35], s91 offen offset:3264 lds\n"
    "v_mfma_f32_16x16x32_bf16 v[80:83], v[248:251], v[56:59], v[80:83]\n"
    "v_min_u32 v107, -2, v107\n"
    "buffer_store_dwordx4 v[104:107], v1, s[40:43], s21 offen sc1\n"
    "v_mfma_f32_16x16x32_bf16 v[84:87], v[252:255], v[56:59], v[84:87]\n"
    "v_cvt_pk_bf16_f32 v112, v76, v77\n"
    "v_cvt_pk_bf16_f32 v113, v78, v79\n"
    "v_mfma_f32_16x16x32_bf16 v[88:91], a[60:63], v[56:59], v[88:91]\n"
    "v_min_u32 v112, -2, v112\n"
    "v_min_u32 v113, -2, v113\n"
    "ds_read_b128 v[108:111], v39 offset:3072\n"
    "s_mul_i32 s7, s62, 0x2c000\n"
    "s_mul_i32 s8, s6, 0x2c00\n"
    "s_add_u32 s7, s7, s8\n"
    "s_add_u32 s7, s7, 0x600000\n"
    "s_cmp_eq_u32 s6, 3\n"
    "s_cselect_b32 s8, 0x40000000, 0\n"
    "v_or_b32 v48, s8, v1\n"
    "s_add_u32 s68, s7, 0\n"
    "s_add_u32 s72, s7, 0x2000\n"
    "s_add_u32 s8, s86, 0\n"
    "s_and_b32 s8, s8, 3\n"
    "s_lshl_b32 s8, s8, 11\n"
    "v_add_u32 v44, s8, v1\n"
    "s_add_u32 s69, s7, 0xb000\n"
    "s_add_u32 s73, s7, 0xd000\n"
    "s_add_u32 s8, s86, 1\n"
    "s_and_b32 s8, s8, 3\n"
    "s_lshl_b32 s8, s8, 11\n"
    "v_add_u32 v45, s8, v1\n"
    "s_add_u32 s70, s7, 0x16000\n"
    "s_add_u32 s74, s7, 0x18000\n"
    "s_add_u32 s8, s86, 2\n"
    "s_and_b32 s8, s8, 3\n"
    "s_lshl_b32 s8, s8, 11\n"
    "v_add_u32 v46, s8, v1\n"
    "s_add_u32 s71, s7, 0x21000\n"
    "s_add_u32 s75, s7, 0x23000\n"
    "s_add_u32 s8, s86, 3\n"
    "s_and_b32 s8, s8, 3\n"
    "s_lshl_b32 s8, s8, 11\n"
    "v_add_u32 v47, s8, v1\n"
    "s_waitcnt lgkmcnt(0)\n"
    "v_mfma_f32_16x16x4_f32 v[80:83], v36, v108, v[80:83]\n"
    "v_mfma_f32_16x16x4_f32 v[84:87], v37, v108, v[84:87]\n"
    "v_mfma_f32_16x16x4_f32 v[88:91], v38, v108, v[88:91]\n"
    "buffer_load_dwordx4 v[128:131], v44, s[40:43], s68 offen sc1\n"
    "s_nop 6\n"
    "v_pk_mul_f32 v[92:93], v[80:81], v[80:81]\n"
    "v_pk_mul_f32 v[94:95], v[82:83], v[82:83]\n"
    "v_pk_mul_f32 v[96:97], v[84:85], v[84:85]\n"
    "v_pk_mul_f32 v[98:99], v[86:87], v[86:87]\n"
    "v_pk_mul_f32 v[100:101], v[88:89], v[88:89]\n"
    "v_pk_mul_f32 v[102:103], v[90:91], v[90:91]\n"
    "v_pk_fma_f32 v[92:93], v[92:93], v[110:111], v[108:109] op_sel:[0,0,1] op_sel_hi:[1,0,1]\n"
    "v_pk_fma_f32 v[94:95], v[94:95], v[110:111], v[108:109] op_sel:[0,0,1] op_sel_hi:[1,0,1]\n"
    "buffer_load_dwordx4 v[132:135], v44, s[40:43], s68 offen offset:1024 sc1\n"
    "v_pk_fma_f32 v[96:97], v[96:97], v[110:111], v[108:109] op_sel:[0,0,1] op_sel_hi:[1,0,1]\n"
    "v_pk_fma_f32 v[98:99], v[98:99], v[110:111], v[108:109] op_sel:[0,0,1] op_sel_hi:[1,0,1]\n"
    "v_pk_fma_f32 v[100:101], v[100:101], v[110:111], v[108:109] op_sel:[0,0,1] op_sel_hi:[1,0,1]\n"
    "v_pk_fma_f32 v[102:103], v[102:103], v[110:111], v[108:109] op_sel:[0,0,1] op_sel_hi:[1,0,1]\n"
    "v_pk_mul_f32 v[92:93], v[92:93], v[80:81]\n"
    "v_pk_mul_f32 v[94:95], v[94:95], v[82:83]\n"
    "v_pk_mul_f32 v[96:97], v[96:97], v[84:85]\n"
    "v_pk_mul_f32 v[98:99], v[98:99], v[86:87]\n"
    "buffer_load_dwordx4 v[136:139], v45, s[40:43], s68 offen sc1\n"
    "v_pk_mul_f32 v[100:101], v[100:101], v[88:89]\n"
    "v_pk_mul_f32 v[102:103], v[102:103], v[90:91]\n"
    "v_exp_f32 v92, v92\n"
    "v_exp_f32 v93, v93\n"
    "v_exp_f32 v94, v94\n"
    "buffer_load_dwordx4 v[140:143], v45, s[40:43], s68 offen offset:1024 sc1\n"
    "v_exp_f32 v95, v95\n"
    "v_exp_f32 v96, v96\n"
    "v_exp_f32 v97, v97\n"
    "v_exp_f32 v98, v98\n"
    "buffer_load_dwordx4 v[144:147], v46, s[40:43], s68 offen sc1\n"
    "v_exp_f32 v99, v99\n"
    "v_exp_f32 v100, v100\n"
    "v_exp_f32 v101, v101\n"
    "v_exp_f32 v102, v102\n"
    "buffer_load_dwordx4 v[148:151], v46, s[40:43], s68 offen offset:1024 sc1\n"
    "v_exp_f32 v103, v103\n"
    "v_pk_fma_f32 v[92:93], v[92:93], v[110:111], v[110:111] op_sel:[0,1,1] op_sel_hi:[1,1,1]\n"
    "v_pk_fma_f32 v[94:95], v[94:95], v[110:111], v[110:111] op_sel:[0,1,1] op_sel_hi:[1,1,1]\n"
    "v_pk_fma_f32 v[96:97], v[96:97], v[110:111], v[110:111] op_sel:[0,1,1] op_sel_hi:[1,1,1]\n"
    "v_pk_fma_f32 v[98:99], v[98:99], v[110:111], v[110:111] op_sel:[0,1,1] op_sel_hi:[1,1,1]\n"
    "v_pk_fma_f32 v[100:101], v[100:101], v[110:111], v[110:111] op_sel:[0,1,1] op_sel_hi:[1,1,1]\n"
    "v_pk_fma_f32 v[102:103], v[102:103], v[110:111], v[110:111] op_sel:[0,1,1] op_sel_hi:[1,1,1]\n"
    "buffer_load_dwordx4 v[152:155], v47, s[40:43], s68 offen sc1\n"
    "v_rcp_f32 v92, v92\n"
    "v_rcp_f32 v93, v93\n"
    "v_rcp_f32 v94, v94\n"
    "v_rcp_f32 v95, v95\n"
    "buffer_load_dwordx4 v[156:159], v47, s[40:43], s68 offen offset:1024 sc1\n"
    "v_rcp_f32 v96, v96\n"
    "v_rcp_f32 v97, v97\n"
    "v_rcp_f32 v98, v98\n"
    "v_rcp_f32 v99, v99\n"
    "v_rcp_f32 v100, v100\n"
    "v_rcp_f32 v101, v101\n"
    "v_rcp_f32 v102, v102\n"
    "v_rcp_f32 v103, v103\n"
    "v_pk_mul_f32 v[80:81], v[80:81], v[92:93]\n"
    "v_pk_mul_f32 v[82:83], v[82:83], v[94:95]\n"
    "v_pk_mul_f32 v[84:85], v[84:85], v[96:97]\n"
    "v_pk_mul_f32 v[86:87], v[86:87], v[98:99]\n"
    "v_pk_mul_f32 v[88:89], v[88:89], v[100:101]\n"
    "v_pk_mul_f32 v[90:91], v[90:91], v[102:103]\n"
    "s_add_u32 s21, s0, 0x21000\n"
    "v_cvt_pk_bf16_f32 v104, v80, v81\n"
    "v_cvt_pk_bf16_f32 v105, v82, v83\n"
    "v_cvt_pk_bf16_f32 v106, v84, v85\n"
    "v_cvt_pk_bf16_f32 v107, v86, v87\n"
    "v_min_u32 v104, -2, v104\n"
    "v_min_u32 v105, -2, v105\n"
    "v_min_u32 v106, -2, v106\n"
    "v_min_u32 v107, -2, v107\n"
    "buffer_store_dwordx4 v[104:107], v1, s[40:43], s21 offen sc1\n"
    "v_cvt_pk_bf16_f32 v114, v88, v89\n"
    "v_cvt_pk_bf16_f32 v115, v90, v91\n"
    "v_min_u32 v114, -2, v114\n"
    "v_min_u32 v115, -2, v115\n"
    "s_nop 0\n"
    "v_permlane16_swap_b32 v112, v114\n"
    "v_permlane16_swap_b32 v113, v115\n"
    "buffer_store_dwordx4 v[112:115], v120, s[40:43], s1 offen sc1\n"
    "buffer_load_dwordx4 v[160:163], v1, s[40:43], s72 offen sc1\n"
    "s_waitcnt vmcnt(30)\n"
    "v_add_u32 v6, 0xffff3400, v6\n"
    "v_add_u32 v7, 0xffff3400, v7\n"
    "v_add_u32 v8, 0xffff3400, v8\n"
    "v_add_u32 v9, 0xffff3400, v9\n"
    "ds_read_b64_tr_b16 a[160:161], v6\n"
    "ds_read_b64_tr_b16 a[162:163], v6 offset:2176\n"
    "ds_read_b64_tr_b16 a[164:165], v7\n"
    "ds_read_b64_tr_b16 a[166:167], v7 offset:2176\n"
    "ds_read_b64_tr_b16 a[168:169], v8\n"
    "ds_read_b64_tr_b16 a[170:171], v8 offset:2176\n"
    "buffer_load_dwordx4 v[164:167], v1, s[40:43], s72 offen offset:1024 sc1\n"
    "ds_read_b64_tr_b16 a[172:173], v9\n"
    "ds_read_b64_tr_b16 a[174:175], v9 offset:2176\n"
    "s_waitcnt vmcnt(27)\n"
    "ds_read_b64_tr_b16 a[176:177], v6 offset:17408\n"
    "ds_read_b64_tr_b16 a[178:179], v6 offset:19584\n"
    "ds_read_b64_tr_b16 a[180:181], v7 offset:17408\n"
    "ds_read_b64_tr_b16 a[182:183], v7 offset:19584\n"
    "ds_read_b64_tr_b16 a[184:185], v8 offset:17408\n"
    "ds_read_b64_tr_b16 a[186:187], v8 offset:19584\n"
    "ds_read_b64_tr_b16 a[188:189], v9 offset:17408\n"
    "ds_read_b64_tr_b16 a[190:191], v9 offset:19584\n"
    "buffer_load_dwordx4 v[168:171], v48, s[40:43], s72 offen offset:2048 sc1\n"
    "s_waitcnt vmcnt(24)\n"
    "ds_read_b64_tr_b16 a[192:193], v2 offset:34816\n"
    "ds_read_b64_tr_b16 a[194:195], v4 offset:35328\n"
    "ds_read_b64_tr_b16 a[196:197], v3 offset:34816\n"
    "ds_read_b64_tr_b16 a[198:199], v5 offset:35328\n"
    "ds_read_b64_tr_b16 a[200:201], v4 offset:34816\n"
    "ds_read_b64_tr_b16 a[202:203], v2 offset:35328\n"
    "ds_read_b64_tr_b16 a[204:205], v5 offset:34816\n"
    "ds_read_b64_tr_b16 a[206:207], v3 offset:35328\n"
    "s_waitcnt vmcnt(18)\n"
    "ds_read_b64_tr_b16 a[208:209], v2 offset:52224\n"
    "ds_read_b64_tr_b16 a[210:211], v4 offset:52736\n"
    "buffer_load_dwordx4 v[172:175], v44, s[40:43], s69 offen sc1\n"
    "ds_read_b64_tr_b16 a[212:213], v3 offset:52224\n"
    "ds_read_b64_tr_b16 a[214:215], v5 offset:52736\n"
    "ds_read_b64_tr_b16 a[216:217], v4 offset:52224\n"
    "ds_read_b64_tr_b16 a[218:219], v2 offset:52736\n"
    "ds_read_b64_tr_b16 a[220:221], v5 offset:52224\n"
    "ds_read_b64_tr_b16 a[222:223], v3 offset:52736\n"
    "s_waitcnt vmcnt(15)\n"
    "v_add_u32 v2, 0xcc00, v2\n"
    "v_add_u32 v3, 0xcc00, v3\n"
    "v_add_u32 v4, 0xcc00, v4\n"
    "v_add_u32 v5, 0xcc00, v5\n"
    "buffer_load_dwordx4 v[176:179], v44, s[40:43], s69 offen offset:1024 sc1\n"
    "ds_read_b64_tr_b16 a[224:225], v2 offset:17408\n"
    "ds_read_b64_tr_b16 a[226:227], v4 offset:17920\n"
    "ds_read_b64_tr_b16 a[228:229], v3 offset:17408\n"
    "ds_read_b64_tr_b16 a[230:231], v5 offset:17920\n"
    "ds_read_b64_tr_b16 a[232:233], v4 offset:17408\n"
    "ds_read_b64_tr_b16 a[234:235], v2 offset:17920\n"
    "ds_read_b64_tr_b16 a[236:237], v5 offset:17408\n"
    "ds_read_b64_tr_b16 a[238:239], v3 offset:17920\n"
    "buffer_load_dwordx4 v[180:183], v45, s[40:43], s69 offen sc1\n"
    "s_lshl_b32 s7, s6, 12\n"
    "v_add_u32 v49, s7, v1\n"
    "s_lshl_b32 s7, s6, 14\n"
    "v_add_u32 v50, s7, v1\n"
    "s_waitcnt lgkmcnt(0)\n"
    "s_barrier\n"
    "s_waitcnt vmcnt(15)\n"
    "v_max3_u32 v51, v128, v129, v130\n"
    "v_max_u32 v51, v51, v131\n"
    "s_waitcnt vmcnt(14)\n"
    "v_max3_u32 v51, v51, v132, v133\n"
    "v_max3_u32 v51, v51, v134, v135\n"
    "s_waitcnt vmcnt(13)\n"
    "v_max3_u32 v51, v51, v136, v137\n"
    "v_max3_u32 v51, v51, v138, v139\n"
    "s_waitcnt vmcnt(12)\n"
    "v_max3_u32 v51, v51, v140, v141\n"
    "v_max3_u32 v51, v51, v142, v143\n"
    "v_cmp_eq_u32 vcc, -1, v51\n"
    "s_nop 0\n"
    "s_cbranch_vccnz L_hv0\n"
    "L_hv0_ok:\n"
    "v_mfma_f32_16x16x32_bf16 v[52:55], a[64:67], v[128:131], 0\n"
    "v_mfma_f32_16x16x32_bf16 v[56:59], a[68:71], v[128:131], 0\n"
    "v_mfma_f32_16x16x32_bf16 v[60:63], a[72:75], v[128:131], 0\n"
    "v_mfma_f32_16x16x32_bf16 v[64:67], a[76:79], v[128:131], 0\n"
    "buffer_load_dwordx4 v[184:187], v45, s[40:43], s69 offen offset:1024 sc1\n"
    "v_mfma_f32_16x16x32_bf16 v[52:55], a[80:83], v[132:135], v[52:55]\n"
    "s_waitcnt vmcnt(12)\n"
    "v_max3_u32 v51, v144, v145, v146\n"
    "v_mfma_f32_16x16x32_bf16 v[56:59], a[84:87], v[132:135], v[56:59]\n"
    "v_max_u32 v51, v51, v147\n"
    "v_mfma_f32_16x16x32_bf16 v[60:63], a[88:91], v[132:135], v[60:63]\n"
    "s_waitcnt vmcnt(11)\n"
    "v_max3_u32 v51, v51, v148, v149\n"
    "v_mfma_f32_16x16x32_bf16 v[64:67], a[92:95], v[132:135], v[64:67]\n"
    "buffer_load_dwordx4 v[188:191], v46, s[40:43], s69 offen sc1\n"
    "v_mfma_f32_16x16x32_bf16 v[52:55], a[96:99], v[136:139], v[52:55]\n"
    "v_max3_u32 v51, v51, v150, v151\n"
    "v_mfma_f32_16x16x32_bf16 v[56:59], a[100:103], v[136:139], v[56:59]\n"
    "s_waitcnt vmcnt(11)\n"
    "v_max3_u32 v51, v51, v152, v153\n"
    "v_mfma_f32_16x16x32_bf16 v[60:63], a[104:107], v[136:139], v[60:63]\n"
    "v_max3_u32 v51, v51, v154, v155\n"
    "v_mfma_f32_16x16x32_bf16 v[64:67], a[108:111], v[136:139], v[64:67]\n"
    "s_waitcnt vmcnt(10)\n"
    "v_max3_u32 v51, v51, v156, v157\n"
    "buffer_load_dwordx4 v[192:195], v46, s[40:43], s69 offen offset:1024 sc1\n"
    "v_mfma_f32_16x16x32_bf16 v[52:55], a[112:115], v[140:143], v[52:55]\n"
    "v_max3_u32 v51, v51, v158, v159\n"
    "v_mfma_f32_16x16x32_bf16 v[56:59], a[116:119], v[140:143], v[56:59]\n"
    "v_cmp_eq_u32 vcc, -1, v51\n"
    "v_mfma_f32_16x16x32_bf16 v[60:63], a[120:123], v[140:143], v[60:63]\n"
    "s_cbranch_vccnz L_hv4\n"
    "L_hv4_ok:\n"
    "v_mfma_f32_16x16x32_bf16 v[64:67], a[124:127], v[140:143], v[64:67]\n"
    "buffer_load_dwordx4 v[196:199], v47, s[40:43], s69 offen sc1\n"
    "v_mfma_f32_16x16x32_bf16 v[52:55], a[128:131], v[144:147], v[52:55]\n"
    "v_mfma_f32_16x16x32_bf16 v[56:59], a[132:135], v[144:147], v[56:59]\n"
    "v_mfma_f32_16x16x32_bf16 v[60:63], a[136:139], v[144:147], v[60:63]\n"
    "v_mfma_f32_16x16x32_bf16 v[64:67], a[140:143], v[144:147], v[64:67]\n"
    "buffer_load_dwordx4 v[200:203], v47, s[40:43], s69 offen offset:1024 sc1\n"
    "v_mfma_f32_16x16x32_bf16 v[52:55], a[144:147], v[148:151], v[52:55]\n"
    "s_waitcnt vmcnt(10)\n"
    "v_max3_u32 v51, v160, v161, v162\n"
    "v_mfma_f32_16x16x32_bf16 v[56:59], a[148:151], v[148:151], v[56:59]\n"
    "v_max_u32 v51, v51, v163\n"
    "v_mfma_f32_16x16x32_bf16 v[60:63], a[152:155], v[148:151], v[60:63]\n"
    "s_waitcnt vmcnt(9)\n"
    "v_max3_u32 v51, v51, v164, v165\n"
    "v_mfma_f32_16x16x32_bf16 v[64:67], a[156:159], v[148:151], v[64:67]\n"
    "buffer_load_dwordx4 v[204:207], v1, s[40:43], s73 offen sc1\n"
    "v_mfma_f32_16x16x32_bf16 v[52:55], a[160:163], v[152:155], v[52:55]\n"
    "v_max3_u32 v51, v51, v166, v167\n"
    "v_mfma_f32_16x16x32_bf16 v[56:59], a[164:167], v[152:155], v[56:59]\n"
    "s_waitcnt vmcnt(9)\n"
    "v_max3_u32 v51, v51, v168, v169\n"
    "v_mfma_f32_16x16x32_bf16 v[60:63], a[168:171], v[152:155], v[60:63]\n"
    "v_max3_u32 v51, v51, v170, v171\n"
    "v_mfma_f32_16x16x32_bf16 v[64:67], a[172:175], v[152:155], v[64:67]\n"
    "s_waitcnt vmcnt(8)\n"
    "v_max3_u32 v51, v51, v172, v173\n"
    "buffer_load_dwordx4 v[208:211], v1, s[40:43], s73 offen offset:1024 sc1\n"
    "v_mfma_f32_16x16x32_bf16 v[52:55], a[176:179], v[156:159], v[52:55]\n"
    "v_max3_u32 v51, v51, v174, v175\n"
    "v_mfma_f32_16x16x32_bf16 v[56:59], a[180:183], v[156:159], v[56:59]\n"
    "v_cmp_eq_u32 vcc, -1, v51\n"
    "v_mfma_f32_16x16x32_bf16 v[60:63], a[184:187], v[156:159], v[60:63]\n"
    "s_cbranch_vccnz L_hv8\n"
    "L_hv8_ok:\n"
    "v_mfma_f32_16x16x32_bf16 v[64:67], a[188:191], v[156:159], v[64:67]\n"
    "buffer_load_dwordx4 v[212:215], v48, s[40:43], s73 offen offset:2048 sc1\n"
    "v_mfma_f32_16x16x32_bf16 v[52:55], a[192:195], v[160:163], v[52:55]\n"
    "v_mfma_f32_16x16x32_bf16 v[56:59], a[196:199], v[160:163], v[56:59]\n"
    "v_mfma_f32_16x16x32_bf16 v[60:63], a[200:203], v[160:163], v[60:63]\n"
    "v_mfma_f32_16x16x32_bf16 v[64:67], a[204:207], v[160:163], v[64:67]\n"
    "buffer_load_dwordx4 v[216:219], v44, s[40:43], s70 offen sc1\n"
    "v_mfma_f32_16x16x32_bf16 v[52:55], a[208:211], v[164:167], v[52:55]\n"
    "s_waitcnt vmcnt(10)\n"
    "v_max3_u32 v51, v176, v177, v178\n"
    "v_mfma_f32_16x16x32_bf16 v[56:59], a[212:215], v[164:167], v[56:59]\n"
    "v_max_u32 v51, v51, v179\n"
    "v_mfma_f32_16x16x32_bf16 v[60:63], a[216:219], v[164:167], v[60:63]\n"
    "s_waitcnt vmcnt(9)\n"
    "v_max3_u32 v51, v51, v180, v181\n"
    "v_mfma_f32_16x16x32_bf16 v[64:67], a[220:223], v[164:167], v[64:67]\n"
    "buffer_load_dwordx4 v[220:223], v44, s[40:43], s70 offen offset:1024 sc1\n"
    "v_mfma_f32_16x16x32_bf16 v[52:55], a[224:227], v[168:171], v[52:55]\n"
    "v_max3_u32 v51, v51, v182, v183\n"
    "v_mfma_f32_16x16x32_bf16 v[56:59], a[228:231], v[168:171], v[56:59]\n"
    "s_waitcnt vmcnt(9)\n"
    "v_max3_u32 v51, v51, v184, v185\n"
    "v_mfma_f32_16x16x32_bf16 v[60:63], a[232:235], v[168:171], v[60:63]\n"
    "v_max3_u32 v51, v51, v186, v187\n"
    "v_mfma_f32_16x16x32_bf16 v[64:67], a[236:239], v[168:171], v[64:67]\n"
    "s_waitcnt vmcnt(8)\n"
    "v_max3_u32 v51, v51, v188, v189\n"
    "buffer_load_dwordx4 v[224:227], v45, s[40:43], s70 offen sc1\n"
    "v_mfma_f32_16x16x32_bf16 v[68:71], a[64:67], v[172:175], 0\n"
    "v_max3_u32 v51, v51, v190, v191\n"
    "v_mfma_f32_16x16x32_bf16 v[72:75], a[68:71], v[172:175], 0\n"
    "v_cmp_eq_u32 vcc, -1, v51\n"
    "v_mfma_f32_16x16x32_bf16 v[76:79], a[72:75], v[172:175], 0\n"
    "s_cbranch_vccnz L_hv12\n"
    "L_hv12_ok:\n"
    "v_mfma_f32_16x16x32_bf16 v[80:83], a[76:79], v[172:175], 0\n"
    "buffer_load_dwordx4 v[228:231], v45, s[40:43], s70 offen offset:1024 sc1\n"
    "v_mfma_f32_16x16x32_bf16 v[68:71], a[80:83], v[176:179], v[68:71]\n"
    "v_mfma_f32_16x16x32_bf16 v[72:75], a[84:87], v[176:179], v[72:75]\n"
    "ds_write_b128 v49, v[52:55]\n"
    "v_mfma_f32_16x16x32_bf16 v[76:79], a[88:91], v[176:179], v[76:79]\n"
    "v_mfma_f32_16x16x32_bf16 v[80:83], a[92:95], v[176:179], v[80:83]\n"
    "buffer_load_dwordx4 v[232:235], v46, s[40:43], s70 offen sc1\n"
    "v_mfma_f32_16x16x32_bf16 v[68:71], a[96:99], v[180:183], v[68:71]\n"
    "s_waitcnt vmcnt(10)\n"
    "v_max3_u32 v51, v192, v193, v194\n"
    "v_mfma_f32_16x16x32_bf16 v[72:75], a[100:103], v[180:183], v[72:75]\n"
    "v_max_u32 v51, v51, v195\n"
    "ds_write_b128 v49, v[56:59] offset:1024\n"
    "v_mfma_f32_16x16x32_bf16 v[76:79], a[104:107], v[180:183], v[76:79]\n"
    "s_waitcnt vmcnt(9)\n"
    "v_max3_u32 v51, v51, v196, v197\n"
    "v_mfma_f32_16x16x32_bf16 v[80:83], a[108:111], v[180:183], v[80:83]\n"
    "buffer_load_dwordx4 v[236:239], v46, s[40:43], s70 offen offset:1024 sc1\n"
    "v_mfma_f32_16x16x32_bf16 v[68:71], a[112:115], v[184:187], v[68:71]\n"
    "v_max3_u32 v51, v51, v198, v199\n"
    "v_mfma_f32_16x16x32_bf16 v[72:75], a[116:119], v[184:187], v[72:75]\n"
    "s_waitcnt vmcnt(9)\n"
    "v_max3_u32 v51, v51, v200, v201\n"
    "ds_write_b128 v49, v[60:63] offset:2048\n"
    "v_mfma_f32_16x16x32_bf16 v[76:79], a[120:123], v[184:187], v[76:79]\n"
    "v_max3_u32 v51, v51, v202, v203\n"
    "v_mfma_f32_16x16x32_bf16 v[80:83], a[124:127], v[184:187], v[80:83]\n"
    "s_waitcnt vmcnt(8)\n"
    "v_max3_u32 v51, v51, v204, v205\n"
    "buffer_load_dwordx4 v[240:243], v47, s[40:43], s70 offen sc1\n"
    "v_mfma_f32_16x16x32_bf16 v[68:71], a[128:131], v[188:191], v[68:71]\n"
    "v_max3_u32 v51, v51, v206, v207\n"
    "v_mfma_f32_16x16x32_bf16 v[72:75], a[132:135], v[188:191], v[72:75]\n"
    "v_cmp_eq_u32 vcc, -1, v51\n"
    "ds_write_b128 v49, v[64:67] offset:3072\n"
    "v_mfma_f32_16x16x32_bf16 v[76:79], a[136:139], v[188:191], v[76:79]\n"
    "s_cbranch_vccnz L_hv16\n"
    "L_hv16_ok:\n"
    "v_mfma_f32_16x16x32_bf16 v[80:83], a[140:143], v[188:191], v[80:83]\n"
    "buffer_load_dwordx4 v[244:247], v47, s[40:43], s70 offen offset:1024 sc1\n"
    "v_mfma_f32_16x16x32_bf16 v[68:71], a[144:147], v[192:195], v[68:71]\n"
    "v_mfma_f32_16x16x32_bf16 v[72:75], a[148:151], v[192:195], v[72:75]\n"
    "v_mfma_f32_16x16x32_bf16 v[76:79], a[152:155], v[192:195], v[76:79]\n"
    "v_mfma_f32_16x16x32_bf16 v[80:83], a[156:159], v[192:195], v[80:83]\n"
    "buffer_load_dwordx4 v[248:251], v1, s[40:43], s74 offen sc1\n"
    "v_mfma_f32_16x16x32_bf16 v[68:71], a[160:163], v[196:199], v[68:71]\n"
    "s_waitcnt vmcnt(10)\n"
    "v_max3_u32 v51, v208, v209, v210\n"
    "v_mfma_f32_16x16x32_bf16 v[72:75], a[164:167], v[196:199], v[72:75]\n"
    "v_max_u32 v51, v51, v211\n"
    "v_mfma_f32_16x16x32_bf16 v[76:79], a[168:171], v[196:199], v[76:79]\n"
    "s_waitcnt vmcnt(9)\n"
    "v_max3_u32 v51, v51, v212, v213\n"
    "v_mfma_f32_16x16x32_bf16 v[80:83], a[172:175], v[196:199], v[80:83]\n"
    "buffer_load_dwordx4 v[252:255], v1, s[40:43], s74 offen offset:1024 sc1\n"
    "v_mfma_f32_16x16x32_bf16 v[68:71], a[176:179], v[200:203], v[68:71]\n"
    "v_max3_u32 v51, v51, v214, v215\n"
    "v_mfma_f32_16x16x32_bf16 v[72:75], a[180:183], v[200:203], v[72:75]\n"
    "s_waitcnt vmcnt(9)\n"
    "v_max3_u32 v51, v51, v216, v217\n"
    "v_mfma_f32_16x16x32_bf16 v[76:79], a[184:187], v[200:203], v[76:79]\n"
    "v_max3_u32 v51, v51, v218, v219\n"
    "v_mfma_f32_16x16x32_bf16 v[80:83], a[188:191], v[200:203], v[80:83]\n"
    "s_waitcnt vmcnt(8)\n"
    "v_max3_u32 v51, v51, v220, v221\n"
    "buffer_load_dwordx4 v[128:131], v48, s[40:43], s74 offen offset:2048 sc1\n"
    "v_mfma_f32_16x16x32_bf16 v[68:71], a[192:195], v[204:207], v[68:71]\n"
    "v_max3_u32 v51, v51, v222, v223\n"
    "v_mfma_f32_16x16x32_bf16 v[72:75], a[196:199], v[204:207], v[72:75]\n"
    "v_cmp_eq_u32 vcc, -1, v51\n"
    "v_mfma_f32_16x16x32_bf16 v[76:79], a[200:203], v[204:207], v[76:79]\n"
    "s_cbranch_vccnz L_hv20\n"
    "L_hv20_ok:\n"
    "v_mfma_f32_16x16x32_bf16 v[80:83], a[204:207], v[204:207], v[80:83]\n"
    "buffer_load_dwordx4 v[132:135], v44, s[40:43], s71 offen sc1\n"
    "v_mfma_f32_16x16x32_bf16 v[68:71], a[208:211], v[208:211], v[68:71]\n"
    "v_mfma_f32_16x16x32_bf16 v[72:75], a[212:215], v[208:211], v[72:75]\n"
    "v_mfma_f32_16x16x32_bf16 v[76:79], a[216:219], v[208:211], v[76:79]\n"
    "v_mfma_f32_16x16x32_bf16 v[80:83], a[220:223], v[208:211], v[80:83]\n"
    "buffer_load_dwordx4 v[136:139], v44, s[40:43], s71 offen offset:1024 sc1\n"
    "v_mfma_f32_16x16x32_bf16 v[68:71], a[224:227], v[212:215], v[68:71]\n"
    "s_waitcnt vmcnt(10)\n"
    "v_max3_u32 v51, v224, v225, v226\n"
    "v_mfma_f32_16x16x32_bf16 v[72:75], a[228:231], v[212:215], v[72:75]\n"
    "v_max_u32 v51, v51, v227\n"
    "v_mfma_f32_16x16x32_bf16 v[76:79], a[232:235], v[212:215], v[76:79]\n"
    "s_waitcnt vmcnt(9)\n"
    "v_max3_u32 v51, v51, v228, v229\n"
    "v_mfma_f32_16x16x32_bf16 v[80:83], a[236:239], v[212:215], v[80:83]\n"
    "buffer_load_dwordx4 v[140:143], v45, s[40:43], s71 offen sc1\n"
    "v_mfma_f32_16x16x32_bf16 v[84:87], a[64:67], v[216:219], 0\n"
    "v_max3_u32 v51, v51, v230, v231\n"
    "v_mfma_f32_16x16x32_bf16 v[88:91], a[68:71], v[216:219], 0\n"
    "s_waitcnt vmcnt(9)\n"
    "v_max3_u32 v51, v51, v232, v233\n"
    "v_mfma_f32_16x16x32_bf16 v[92:95], a[72:75], v[216:219], 0\n"
    "v_max3_u32 v51, v51, v234, v235\n"
    "v_mfma_f32_16x16x32_bf16 v[96:99], a[76:79], v[216:219], 0\n"
    "s_waitcnt vmcnt(8)\n"
    "v_max3_u32 v51, v51, v236, v237\n"
    "buffer_load_dwordx4 v[144:147], v45, s[40:43], s71 offen offset:1024 sc1\n"
    "v_mfma_f32_16x16x32_bf16 v[84:87], a[80:83], v[220:223], v[84:87]\n"
    "v_max3_u32 v51, v51, v238, v239\n"
    "v_mfma_f32_16x16x32_bf16 v[88:91], a[84:87], v[220:223], v[88:91]\n"
    "v_cmp_eq_u32 vcc, -1, v51\n"
    "ds_write_b128 v49, v[68:71] offset:16384\n"
    "v_mfma_f32_16x16x32_bf16 v[92:95], a[88:91], v[220:223], v[92:95]\n"
    "s_cbranch_vccnz L_hv24\n"
    "L_hv24_ok:\n"
    "v_mfma_f32_16x16x32_bf16 v[96:99], a[92:95], v[220:223], v[96:99]\n"
    "buffer_load_dwordx4 v[148:151], v46, s[40:43], s71 offen sc1\n"
    "v_mfma_f32_16x16x32_bf16 v[84:87], a[96:99], v[224:227], v[84:87]\n"
    "v_mfma_f32_16x16x32_bf16 v[88:91], a[100:103], v[224:227], v[88:91]\n"
    "ds_write_b128 v49, v[72:75] offset:17408\n"
    "v_mfma_f32_16x16x32_bf16 v[92:95], a[104:107], v[224:227], v[92:95]\n"
    "v_mfma_f32_16x16x32_bf16 v[96:99], a[108:111], v[224:227], v[96:99]\n"
    "buffer_load_dwordx4 v[152:155], v46, s[40:43], s71 offen offset:1024 sc1\n"
    "v_mfma_f32_16x16x32_bf16 v[84:87], a[112:115], v[228:231], v[84:87]\n"
    "s_waitcnt vmcnt(10)\n"
    "v_max3_u32 v51, v240, v241, v242\n"
    "v_mfma_f32_16x16x32_bf16 v[88:91], a[116:119], v[228:231], v[88:91]\n"
    "v_max_u32 v51, v51, v243\n"
    "ds_write_b128 v49, v[76:79] offset:18432\n"
    "v_mfma_f32_16x16x32_bf16 v[92:95], a[120:123], v[228:231], v[92:95]\n"
    "s_waitcnt vmcnt(9)\n"
    "v_max3_u32 v51, v51, v244, v245\n"
    "v_mfma_f32_16x16x32_bf16 v[96:99], a[124:127], v[228:231], v[96:99]\n"
    "buffer_load_dwordx4 v[156:159], v47, s[40:43], s71 offen sc1\n"
    "v_mfma_f32_16x16x32_bf16 v[84:87], a[128:131], v[232:235], v[84:87]\n"
    "v_max3_u32 v51, v51, v246, v247\n"
    "v_mfma_f32_16x16x32_bf16 v[88:91], a[132:135], v[232:235], v[88:91]\n"
    "s_waitcnt vmcnt(9)\n"
    "v_max3_u32 v51, v51, v248, v249\n"
    "ds_write_b128 v49, v[80:83] offset:19456\n"
    "v_mfma_f32_16x16x32_bf16 v[92:95], a[136:139], v[232:235], v[92:95]\n"
    "v_max3_u32 v51, v51, v250, v251\n"
    "v_mfma_f32_16x16x32_bf16 v[96:99], a[140:143], v[232:235], v[96:99]\n"
    "s_waitcnt vmcnt(8)\n"
    "v_max3_u32 v51, v51, v252, v253\n"
    "buffer_load_dwordx4 v[160:163], v47, s[40:43], s71 offen offset:1024 sc1\n"
    "v_mfma_f32_16x16x32_bf16 v[84:87], a[144:147], v[236:239], v[84:87]\n"
    "v_max3_u32 v51, v51, v254, v255\n"
    "v_mfma_f32_16x16x32_bf16 v[88:91], a[148:151], v[236:239], v[88:91]\n"
    "v_cmp_eq_u32 vcc, -1, v51\n"
    "v_mfma_f32_16x16x32_bf16 v[92:95], a[152:155], v[236:239], v[92:95]\n"
    "s_cbranch_vccnz L_hv28\n"
    "L_hv28_ok:\n"
    "v_mfma_f32_16x16x32_bf16 v[96:99], a[156:159], v[236:239], v[96:99]\n"
    "buffer_load_dwordx4 v[164:167], v1, s[40:43], s75 offen sc1\n"
    "v_mfma_f32_16x16x32_bf16 v[84:87], a[160:163], v[240:243], v[84:87]\n"
    "ds_read_b128 v[192:195], v124 offset:1024\n"
    "v_mfma_f32_16x16x32_bf16 v[88:91], a[164:167], v[240:243], v[88:91]\n"
    "ds_read_b128 v[196:199], v124 offset:1088\n"
    "v_mfma_f32_16x16x32_bf16 v[92:95], a[168:171], v[240:243], v[92:95]\n"
    "v_mfma_f32_16x16x32_bf16 v[96:99], a[172:175], v[240:243], v[96:99]\n"
    "ds_read_b128 v[200:203], v124 offset:1152\n"
    "buffer_load_dwordx4 v[168:171], v1, s[40:43], s75 offen offset:1024 sc1\n"
    "v_mfma_f32_16x16x32_bf16 v[84:87], a[176:179], v[244:247], v[84:87]\n"
    "s_waitcnt vmcnt(10)\n"
    "v_max3_u32 v51, v128, v129, v130\n"
    "v_mfma_f32_16x16x32_bf16 v[88:91], a[180:183], v[244:247], v[88:91]\n"
    "v_max_u32 v51, v51, v131\n"
    "ds_read_b128 v[204:207], v124 offset:1216\n"
    "v_mfma_f32_16x16x32_bf16 v[92:95], a[184:187], v[244:247], v[92:95]\n"
    "s_waitcnt vmcnt(9)\n"
    "v_max3_u32 v51, v51, v132, v133\n"
    "v_mfma_f32_16x16x32_bf16 v[96:99], a[188:191], v[244:247], v[96:99]\n"
    "buffer_load_dwordx4 v[172:175], v48, s[40:43], s75 offen offset:2048 sc1\n"
    "v_mfma_f32_16x16x32_bf16 v[84:87], a[192:195], v[248:251], v[84:87]\n"
    "v_max3_u32 v51, v51, v134, v135\n"
    "ds_read_b128 v[176:179], v124 offset:768\n"
    "v_mfma_f32_16x16x32_bf16 v[88:91], a[196:199], v[248:251], v[88:91]\n"
    "s_waitcnt vmcnt(9)\n"
    "v_max3_u32 v51, v51, v136, v137\n"
    "v_mfma_f32_16x16x32_bf16 v[92:95], a[200:203], v[248:251], v[92:95]\n"
    "v_max3_u32 v51, v51, v138, v139\n"
    "ds_read_b128 v[180:183], v124 offset:832\n"
    "v_mfma_f32_16x16x32_bf16 v[96:99], a[204:207], v[248:251], v[96:99]\n"
    "s_waitcnt vmcnt(8)\n"
    "v_max3_u32 v51, v51, v140, v141\n"
    "v_mfma_f32_16x16x32_bf16 v[84:87], a[208:211], v[252:255], v[84:87]\n"
    "ds_read_b128 v[184:187], v124 offset:896\n"
    "v_mfma_f32_16x16x32_bf16 v[88:91], a[212:215], v[252:255], v[88:91]\n"
    "v_max3_u32 v51, v51, v142, v143\n"
    "v_mfma_f32_16x16x32_bf16 v[92:95], a[216:219], v[252:255], v[92:95]\n"
    "v_cmp_eq_u32 vcc, -1, v51\n"
    "ds_read_b128 v[188:191], v124 offset:960\n"
    "v_mfma_f32_16x16x32_bf16 v[96:99], a[220:223], v[252:255], v[96:99]\n"
    "s_cbranch_vccnz L_hv32\n"
    "L_hv32_ok:\n"
    "v_mfma_f32_16x16x32_bf16 v[84:87], a[224:227], v[128:131], v[84:87]\n"
    "v_mfma_f32_16x16x32_bf16 v[88:91], a[228:231], v[128:131], v[88:91]\n"
    "v_mfma_f32_16x16x32_bf16 v[92:95], a[232:235], v[128:131], v[92:95]\n"
    "s_waitcnt lgkmcnt(3)\n"
    "v_fma_f32 v20, v192, v176, v20\n"
    "v_mfma_f32_16x16x32_bf16 v[96:99], a[236:239], v[128:131], v[96:99]\n"
    "v_mfma_f32_16x16x32_bf16 v[100:103], a[64:67], v[132:135], 0\n"
    "s_waitcnt vmcnt(7)\n"
    "v_max3_u32 v51, v144, v145, v146\n"
    "v_mfma_f32_16x16x32_bf16 v[104:107], a[68:71], v[132:135], 0\n"
    "v_max_u32 v51, v51, v147\n"
    "v_fma_f32 v21, v193, v177, v21\n"
    "v_mfma_f32_16x16x32_bf16 v[108:111], a[72:75], v[132:135], 0\n"
    "s_waitcnt vmcnt(6)\n"
    "v_max3_u32 v51, v51, v148, v149\n"
    "v_mfma_f32_16x16x32_bf16 v[112:115], a[76:79], v[132:135], 0\n"
    "v_mfma_f32_16x16x32_bf16 v[100:103], a[80:83], v[136:139], v[100:103]\n"
    "v_max3_u32 v51, v51, v150, v151\n"
    "v_fma_f32 v22, v194, v178, v22\n"
    "v_mfma_f32_16x16x32_bf16 v[104:107], a[84:87], v[136:139], v[104:107]\n"
    "s_waitcnt vmcnt(5)\n"
    "v_max3_u32 v51, v51, v152, v153\n"
    "ds_write_b128 v49, v[84:87] offset:32768\n"
    "v_mfma_f32_16x16x32_bf16 v[108:111], a[88:91], v[136:139], v[108:111]\n"
    "v_max3_u32 v51, v51, v154, v155\n"
    "v_mfma_f32_16x16x32_bf16 v[112:115], a[92:95], v[136:139], v[112:115]\n"
    "s_waitcnt vmcnt(4)\n"
    "v_max3_u32 v51, v51, v156, v157\n"
    "v_mfma_f32_16x16x32_bf16 v[100:103], a[96:99], v[140:143], v[100:103]\n"
    "v_max3_u32 v51, v51, v158, v159\n"
    "v_mfma_f32_16x16x32_bf16 v[104:107], a[100:103], v[140:143], v[104:107]\n"
    "ds_write_b128 v49, v[88:91] offset:33792\n"
    "v_fma_f32 v23, v195, v179, v23\n"
    "v_mfma_f32_16x16x32_bf16 v[108:111], a[104:107], v[140:143], v[108:111]\n"
    "v_cmp_eq_u32 vcc, -1, v51\n"
    "v_mfma_f32_16x16x32_bf16 v[112:115], a[108:111], v[140:143], v[112:115]\n"
    "s_cbranch_vccnz L_hv36\n"
    "L_hv36_ok:\n"
    "v_mfma_f32_16x16x32_bf16 v[100:103], a[112:115], v[144:147], v[100:103]\n"
    "s_waitcnt lgkmcnt(4)\n"
    "v_fma_f32 v24, v196, v180, v24\n"
    "v_mfma_f32_16x16x32_bf16 v[104:107], a[116:119], v[144:147], v[104:107]\n"
    "ds_write_b128 v49, v[92:95] offset:34816\n"
    "v_mfma_f32_16x16x32_bf16 v[108:111], a[120:123], v[144:147], v[108:111]\n"
    "v_fma_f32 v25, v197, v181, v25\n"
    "v_mfma_f32_16x16x32_bf16 v[112:115], a[124:127], v[144:147], v[112:115]\n"
    "v_mfma_f32_16x16x32_bf16 v[100:103], a[128:131], v[148:151], v[100:103]\n"
    "s_waitcnt vmcnt(3)\n"
    "v_max3_u32 v51, v160, v161, v162\n"
    "v_mfma_f32_16x16x32_bf16 v[104:107], a[132:135], v[148:151], v[104:107]\n"
    "v_max_u32 v51, v51, v163\n"
    "ds_write_b128 v49, v[96:99] offset:35840\n"
    "v_mfma_f32_16x16x32_bf16 v[108:111], a[136:139], v[148:151], v[108:111]\n"
    "s_waitcnt vmcnt(2)\n"
    "v_max3_u32 v51, v51, v164, v165\n"
    "v_fma_f32 v26, v198, v182, v26\n"
    "v_mfma_f32_16x16x32_bf16 v[112:115], a[140:143], v[148:151], v[112:115]\n"
    "v_mfma_f32_16x16x32_bf16 v[100:103], a[144:147], v[152:155], v[100:103]\n"
    "v_max3_u32 v51, v51, v166, v167\n"
    "v_mfma_f32_16x16x32_bf16 v[104:107], a[148:151], v[152:155], v[104:107]\n"
    "s_waitcnt vmcnt(1)\n"
    "v_max3_u32 v51, v51, v168, v169\n"
    "v_fma_f32 v27, v199, v183, v27\n"
    "v_mfma_f32_16x16x32_bf16 v[108:111], a[152:155], v[152:155], v[108:111]\n"
    "v_max3_u32 v51, v51, v170, v171\n"
    "v_mfma_f32_16x16x32_bf16 v[112:115], a[156:159], v[152:155], v[112:115]\n"
    "s_waitcnt vmcnt(0)\n"
    "v_max3_u32 v51, v51, v172, v173\n"
    "v_mfma_f32_16x16x32_bf16 v[100:103], a[160:163], v[156:159], v[100:103]\n"
    "v_max3_u32 v51, v51, v174, v175\n"
    "s_waitcnt lgkmcnt(5)\n"
    "v_fma_f32 v28, v200, v184, v28\n"
    "v_mfma_f32_16x16x32_bf16 v[104:107], a[164:167], v[156:159], v[104:107]\n"
    "v_mfma_f32_16x16x32_bf16 v[108:111], a[168:171], v[156:159], v[108:111]\n"
    "v_cmp_eq_u32 vcc, -1, v51\n"
    "v_mfma_f32_16x16x32_bf16 v[112:115], a[172:175], v[156:159], v[112:115]\n"
    "s_cbranch_vccnz L_hv40\n"
    "L_hv40_ok:\n"
    "v_fma_f32 v29, v201, v185, v29\n"
    "v_mfma_f32_16x16x32_bf16 v[100:103], a[176:179], v[160:163], v[100:103]\n"
    "v_mfma_f32_16x16x32_bf16 v[104:107], a[180:183], v[160:163], v[104:107]\n"
    "v_fma_f32 v30, v202, v186, v30\n"
    "v_mfma_f32_16x16x32_bf16 v[108:111], a[184:187], v[160:163], v[108:111]\n"
    "v_mfma_f32_16x16x32_bf16 v[112:115], a[188:191], v[160:163], v[112:115]\n"
    "v_fma_f32 v31, v203, v187, v31\n"
    "v_mfma_f32_16x16x32_bf16 v[100:103], a[192:195], v[164:167], v[100:103]\n"
    "v_mfma_f32_16x16x32_bf16 v[104:107], a[196:199], v[164:167], v[104:107]\n"
    "s_waitcnt lgkmcnt(4)\n"
    "v_fma_f32 v32, v204, v188, v32\n"
    "v_mfma_f32_16x16x32_bf16 v[108:111], a[200:203], v[164:167], v[108:111]\n"
    "v_mfma_f32_16x16x32_bf16 v[112:115], a[204:207], v[164:167], v[112:115]\n"
    "v_fma_f32 v33, v205, v189, v33\n"
    "v_mfma_f32_16x16x32_bf16 v[100:103], a[208:211], v[168:171], v[100:103]\n"
    "v_mfma_f32_16x16x32_bf16 v[104:107], a[212:215], v[168:171], v[104:107]\n"
    "v_fma_f32 v34, v206, v190, v34\n"
    "v_mfma_f32_16x16x32_bf16 v[108:111], a[216:219], v[168:171], v[108:111]\n"
    "v_mfma_f32_16x16x32_bf16 v[112:115], a[220:223], v[168:171], v[112:115]\n"
    "v_fma_f32 v35, v207, v191, v35\n"
    "v_mfma_f32_16x16x32_bf16 v[100:103], a[224:227], v[172:175], v[100:103]\n"
    "v_mfma_f32_16x16x32_bf16 v[104:107], a[228:231], v[172:175], v[104:107]\n"
    "v_mfma_f32_16x16x32_bf16 v[108:111], a[232:235], v[172:175], v[108:111]\n"
    "v_mfma_f32_16x16x32_bf16 v[112:115], a[236:239], v[172:175], v[112:115]\n"
    "s_nop 4\n"
    "ds_write_b128 v49, v[100:103] offset:49152\n"
    "ds_write_b128 v49, v[104:107] offset:50176\n"
    "ds_write_b128 v49, v[108:111] offset:51200\n"
    "ds_write_b128 v49, v[112:115] offset:52224\n"
    "s_waitcnt lgkmcnt(0)\n"
    "s_barrier\n"
    "ds_read_b128 v[52:55], v50\n"
    "ds_read_b128 v[56:59], v50 offset:1024\n"
    "ds_read_b128 v[68:71], v50 offset:4096\n"
    "ds_read_b128 v[72:75], v50 offset:5120\n"
    "ds_read_b128 v[84:87], v50 offset:8192\n"
    "ds_read_b128 v[88:91], v50 offset:9216\n"
    "ds_read_b128 v[100:103], v50 offset:12288\n"
    "ds_read_b128 v[104:107], v50 offset:13312\n"
    "ds_read_b128 v[60:63], v50 offset:2048\n"
    "ds_read_b128 v[64:67], v50 offset:3072\n"
    "ds_read_b128 v[76:79], v50 offset:6144\n"
    "ds_read_b128 v[80:83], v50 offset:7168\n"
    "ds_read_b128 v[92:95], v50 offset:10240\n"
    "ds_read_b128 v[96:99], v50 offset:11264\n"
    "ds_read_b128 v[108:111], v50 offset:14336\n"
    "ds_read_b128 v[112:115], v50 offset:15360\n"
    "s_waitcnt lgkmcnt(13)\n"
    "v_pk_add_f32 v[52:53], v[52:53], v[68:69]\n"
    "v_pk_add_f32 v[54:55], v[54:55], v[70:71]\n"
    "s_waitcnt lgkmcnt(12)\n"
    "v_pk_add_f32 v[56:57], v[56:57], v[72:73]\n"
    "v_pk_add_f32 v[58:59], v[58:59], v[74:75]\n"
    "s_waitcnt lgkmcnt(9)\n"
    "v_pk_add_f32 v[84:85], v[84:85], v[100:101]\n"
    "v_pk_add_f32 v[86:87], v[86:87], v[102:103]\n"
    "s_waitcnt lgkmcnt(8)\n"
    "v_pk_add_f32 v[88:89], v[88:89], v[104:105]\n"
    "v_pk_add_f32 v[90:91], v[90:91], v[106:107]\n"
    "v_pk_add_f32 v[52:53], v[52:53], v[84:85]\n"
    "v_pk_add_f32 v[54:55], v[54:55], v[86:87]\n"
    "v_pk_add_f32 v[56:57], v[56:57], v[88:89]\n"
    "v_pk_add_f32 v[58:59], v[58:59], v[90:91]\n"
    "v_pk_fma_f32 v[52:53], v[52:53], v[176:177], v[20:21]\n"
    "v_pk_fma_f32 v[54:55], v[54:55], v[178:179], v[22:23]\n"
    "v_pk_fma_f32 v[56:57], v[56:57], v[180:181], v[24:25]\n"
    "v_pk_fma_f32 v[58:59], v[58:59], v[182:183], v[26:27]\n"
    "v_cvt_pk_bf16_f32 v128, v52, v53\n"
    "v_cvt_pk_bf16_f32 v129, v54, v55\n"
    "v_cvt_pk_bf16_f32 v130, v56, v57\n"
    "v_cvt_pk_bf16_f32 v131, v58, v59\n"
    "s_nop 0\n"
    "v_permlane16_swap_b32 v128, v130\n"
    "v_permlane16_swap_b32 v129, v131\n"
    "buffer_store_dwordx4 v[128:131], v19, s[44:47], s84 offen\n"
    "s_waitcnt lgkmcnt(5)\n"
    "v_pk_add_f32 v[60:61], v[60:61], v[76:77]\n"
    "v_pk_add_f32 v[62:63], v[62:63], v[78:79]\n"
    "s_waitcnt lgkmcnt(4)\n"
    "v_pk_add_f32 v[64:65], v[64:65], v[80:81]\n"
    "v_pk_add_f32 v[66:67], v[66:67], v[82:83]\n"
    "s_waitcnt lgkmcnt(1)\n"
    "v_pk_add_f32 v[92:93], v[92:93], v[108:109]\n"
    "v_pk_add_f32 v[94:95], v[94:95], v[110:111]\n"
    "s_waitcnt lgkmcnt(0)\n"
    "v_pk_add_f32 v[96:97], v[96:97], v[112:113]\n"
    "v_pk_add_f32 v[98:99], v[98:99], v[114:115]\n"
    "v_pk_add_f32 v[60:61], v[60:61], v[92:93]\n"
    "v_pk_add_f32 v[62:63], v[62:63], v[94:95]\n"
    "v_pk_add_f32 v[64:65], v[64:65], v[96:97]\n"
    "v_pk_add_f32 v[66:67], v[66:67], v[98:99]\n"
    "v_pk_fma_f32 v[60:61], v[60:61], v[184:185], v[28:29]\n"
    "v_pk_fma_f32 v[62:63], v[62:63], v[186:187], v[30:31]\n"
    "v_pk_fma_f32 v[64:65], v[64:65], v[188:189], v[32:33]\n"
    "v_pk_fma_f32 v[66:67], v[66:67], v[190:191], v[34:35]\n"
    "v_cvt_pk_bf16_f32 v132, v60, v61\n"
    "v_cvt_pk_bf16_f32 v133, v62, v63\n"
    "v_cvt_pk_bf16_f32 v134, v64, v65\n"
    "v_cvt_pk_bf16_f32 v135, v66, v67\n"
    "s_nop 0\n"
    "v_permlane16_swap_b32 v132, v134\n"
    "v_permlane16_swap_b32 v133, v135\n"
    "buffer_store_dwordx4 v[132:135], v19, s[44:47], s84 offen offset:64\n"
    "s_endpgm\n"
    "L_g1:\n"
    "s_nop 7\n"
    "s_sleep 6\n"
    "s_add_u32 s92, s92, 1\n"
    "s_cmp_gt_u32 s92, 0x20000\n"
    "s_cbranch_scc1 L_g1_ok\n"
    "s_waitcnt vmcnt(0) lgkmcnt(0)\n"
    "s_mov_b32 s11, 0x60000\n"
    "buffer_load_dwordx4 v[40:43], v31, s[40:43], s11 offen sc1\n"
    "buffer_load_dwordx4 v[44:47], v31, s[40:43], s11 offen offset:1024 sc1\n"
    "s_mov_b32 s11, 0x66000\n"
    "buffer_load_dwordx4 v[48:51], v31, s[40:43], s11 offen sc1\n"
    "buffer_load_dwordx4 v[52:55], v31, s[40:43], s11 offen offset:1024 sc1\n"
    "s_mov_b32 s11, 0x6c000\n"
    "buffer_load_dwordx4 v[80:83], v31, s[40:43], s11 offen sc1\n"
    "buffer_load_dwordx4 v[84:87], v31, s[40:43], s11 offen offset:1024 sc1\n"
    "s_mov_b32 s11, 0x72000\n"
    "buffer_load_dwordx4 v[88:91], v31, s[40:43], s11 offen sc1\n"
    "buffer_load_dwordx4 v[92:95], v31, s[40:43], s11 offen offset:1024 sc1\n"
    "s_waitcnt vmcnt(7)\n"
    "v_xor_b32 v56, s67, v41\n"
    "v_bitop3_b32 v56, v56, v43, s67 bitop3:0xf6\n"
    "s_waitcnt vmcnt(6)\n"
    "v_bitop3_b32 v56, v56, v45, s67 bitop3:0xf6\n"
    "v_bitop3_b32 v56, v56, v47, s67 bitop3:0xf6\n"
    "s_waitcnt vmcnt(5)\n"
    "v_bitop3_b32 v56, v56, v49, s67 bitop3:0xf6\n"
    "v_bitop3_b32 v56, v56, v51, s67 bitop3:0xf6\n"
    "s_waitcnt vmcnt(4)\n"
    "v_bitop3_b32 v56, v56, v53, s67 bitop3:0xf6\n"
    "v_bitop3_b32 v56, v56, v55, s67 bitop3:0xf6\n"
    "s_waitcnt vmcnt(3)\n"
    "v_bitop3_b32 v56, v56, v81, s67 bitop3:0xf6\n"
    "v_bitop3_b32 v56, v56, v83, s67 bitop3:0xf6\n"
    "s_waitcnt vmcnt(2)\n"
    "v_bitop3_b32 v56, v56, v85, s67 bitop3:0xf6\n"
    "v_bitop3_b32 v56, v56, v87, s67 bitop3:0xf6\n"
    "s_waitcnt vmcnt(1)\n"
    "v_bitop3_b32 v56, v56, v89, s67 bitop3:0xf6\n"
    "v_bitop3_b32 v56, v56, v91, s67 bitop3:0xf6\n"
    "s_waitcnt vmcnt(0)\n"
    "v_bitop3_b32 v56, v56, v93, s67 bitop3:0xf6\n"
    "v_bitop3_b32 v56, v56, v95, s67 bitop3:0xf6\n"
    "v_cmp_ne_u32 vcc, 0, v56\n"
    "s_nop 0\n"
    "s_cbranch_vccnz L_g1\n"
    "s_nop 7\n"
    "s_branch L_g1_ok\n"
    "L_fo:\n"
    "s_nop 7\n"
    "s_sleep 2\n"
    "s_add_u32 s92, s92, 1\n"
    "s_cmp_gt_u32 s92, 0x20000\n"
    "s_cbranch_scc1 L_fo_ok\n"
    "s_waitcnt vmcnt(0) lgkmcnt(0)\n"
    "buffer_load_dword v36, v37, s[40:43], s100 offen sc1\n"
    "s_waitcnt vmcnt(0)\n"
    "v_cmp_ne_u32 vcc, s67, v36\n"
    "s_nop 0\n"
    "s_cbranch_vccnz L_fo\n"
    "s_nop 7\n"
    "s_branch L_fo_ok\n"
    "L_g3:\n"
    "s_nop 7\n"
    "s_sleep 6\n"
    "s_add_u32 s92, s92, 1\n"
    "s_cmp_gt_u32 s92, 0x20000\n"
    "s_cbranch_scc1 L_g3_ok\n"
    "s_waitcnt vmcnt(0) lgkmcnt(0)\n"
    "s_mov_b32 s11, 0x62000\n"
    "buffer_load_dwordx4 v[92:95], v126, s[40:43], s11 offen sc1\n"
    "s_mov_b32 s11, 0x6e000\n"
    "buffer_load_dwordx4 v[96:99], v126, s[40:43], s11 offen sc1\n"
    "s_waitcnt vmcnt(1)\n"
    "v_xor_b32 v109, s67, v93\n"
    "v_bitop3_b32 v109, v109, v95, s67 bitop3:0xf6\n"
    "s_waitcnt vmcnt(0)\n"
    "v_bitop3_b32 v109, v109, v97, s67 bitop3:0xf6\n"
    "v_bitop3_b32 v109, v109, v99, s67 bitop3:0xf6\n"
    "v_cmp_ne_u32 vcc, 0, v109\n"
    "s_nop 0\n"
    "s_cbranch_vccnz L_g3\n"
    "s_nop 7\n"
    "s_branch L_g3_ok\n"
    "L_g2:\n"
    "s_nop 7\n"
    "s_sleep 6\n"
    "s_add_u32 s92, s92, 1\n"
    "s_cmp_gt_u32 s92, 0x20000\n"
    "s_cbranch_scc1 L_g2_ok\n"
    "s_waitcnt vmcnt(0) lgkmcnt(0)\n"
    "buffer_load_dwordx4 v[116:119], v1, s[40:43], s21 offen sc1\n"
    "s_waitcnt vmcnt(0)\n"
    "v_xor_b32 v96, s67, v117\n"
    "v_bitop3_b32 v96, v96, v119, s67 bitop3:0xf6\n"
    "v_cmp_ne_u32 vcc, 0, v96\n"
    "s_nop 0\n"
    "s_cbranch_vccnz L_g2\n"
    "s_nop 7\n"
    "s_branch L_g2_ok\n"
    "L_bump:\n"
    "s_nop 7\n"
    "s_sleep 6\n"
    "s_add_u32 s92, s92, 1\n"
    "s_cmp_gt_u32 s92, 0x20000\n"
    "s_cbranch_scc1 L_bump_ok\n"
    "s_waitcnt vmcnt(0) lgkmcnt(0)\n"
    "s_mov_b32 s7, 0x1000\n"
    "buffer_load_dwordx4 v[116:119], v1, s[40:43], s7 offen sc1\n"
    "s_waitcnt vmcnt(0)\n"
    "v_xor_b32 v116, s67, v116\n"
    "v_bitop3_b32 v116, v116, v117, s67 bitop3:0xf6\n"
    "v_bitop3_b32 v116, v116, v118, s67 bitop3:0xf6\n"
    "v_bitop3_b32 v116, v116, v119, s67 bitop3:0xf6\n"
    "v_cmp_ne_u32 vcc, 0, v116\n"
    "s_nop 0\n"
    "s_cbranch_vccnz L_bump\n"
    "s_nop 7\n"
    "s_branch L_bump_ok\n"
    "L_fu:\n"
    "s_nop 7\n"
    "s_sleep 2\n"
    "s_add_u32 s92, s92, 1\n"
    "s_cmp_gt_u32 s92, 0x20000\n"
    "s_cbranch_scc1 L_fu_ok\n"
    "s_waitcnt vmcnt(0) lgkmcnt(0)\n"
    "buffer_load_dword v40, v41, s[40:43], s100 offen sc1\n"
    "s_waitcnt vmcnt(0)\n"
    "v_cmp_ne_u32 vcc, s67, v40\n"
    "s_nop 0\n"
    "s_cbranch_vccnz L_fu\n"
    "s_nop 7\n"
    "s_branch L_fu_ok\n"
    "L_hv0:\n"
    "s_nop 7\n"
    "s_sleep 6\n"
    "s_add_u32 s92, s92, 1\n"
    "s_cmp_gt_u32 s92, 0x20000\n"
    "s_cbranch_scc1 L_hv0_ok\n"
    "s_waitcnt vmcnt(0) lgkmcnt(0)\n"
    "buffer_load_dwordx4 v[128:131], v44, s[40:43], s68 offen sc1\n"
    "buffer_load_dwordx4 v[132:135], v44, s[40:43], s68 offen offset:1024 sc1\n"
    "buffer_load_dwordx4 v[136:139], v45, s[40:43], s68 offen sc1\n"
    "buffer_load_dwordx4 v[140:143], v45, s[40:43], s68 offen offset:1024 sc1\n"
    "s_waitcnt vmcnt(3)\n"
    "v_max3_u32 v51, v128, v129, v130\n"
    "v_max_u32 v51, v51, v131\n"
    "s_waitcnt vmcnt(2)\n"
    "v_max3_u32 v51, v51, v132, v133\n"
    "v_max3_u32 v51, v51, v134, v135\n"
    "s_waitcnt vmcnt(1)\n"
    "v_max3_u32 v51, v51, v136, v137\n"
    "v_max3_u32 v51, v51, v138, v139\n"
    "s_waitcnt vmcnt(0)\n"
    "v_max3_u32 v51, v51, v140, v141\n"
    "v_max3_u32 v51, v51, v142, v143\n"
    "v_cmp_eq_u32 vcc, -1, v51\n"
    "s_nop 0\n"
    "s_cbranch_vccnz L_hv0\n"
    "s_nop 7\n"
    "s_branch L_hv0_ok\n"
    "L_hv4:\n"
    "s_nop 7\n"
    "s_sleep 6\n"
    "s_add_u32 s92, s92, 1\n"
    "s_cmp_gt_u32 s92, 0x20000\n"
    "s_cbranch_scc1 L_hv4_ok\n"
    "s_waitcnt vmcnt(0) lgkmcnt(0)\n"
    "buffer_load_dwordx4 v[144:147], v46, s[40:43], s68 offen sc1\n"
    "buffer_load_dwordx4 v[148:151], v46, s[40:43], s68 offen offset:1024 sc1\n"
    "buffer_load_dwordx4 v[152:155], v47, s[40:43], s68 offen sc1\n"
    "buffer_load_dwordx4 v[156:159], v47, s[40:43], s68 offen offset:1024 sc1\n"
    "s_waitcnt vmcnt(3)\n"
    "v_max3_u32 v51, v144, v145, v146\n"
    "v_max_u32 v51, v51, v147\n"
    "s_waitcnt vmcnt(2)\n"
    "v_max3_u32 v51, v51, v148, v149\n"
    "v_max3_u32 v51, v51, v150, v151\n"
    "s_waitcnt vmcnt(1)\n"
    "v_max3_u32 v51, v51, v152, v153\n"
    "v_max3_u32 v51, v51, v154, v155\n"
    "s_waitcnt vmcnt(0)\n"
    "v_max3_u32 v51, v51, v156, v157\n"
    "v_max3_u32 v51, v51, v158, v159\n"
    "v_cmp_eq_u32 vcc, -1, v51\n"
    "s_nop 0\n"
    "s_cbranch_vccnz L_hv4\n"
    "s_nop 7\n"
    "s_branch L_hv4_ok\n"
    "L_hv8:\n"
    "s_nop 7\n"
    "s_sleep 6\n"
    "s_add_u32 s92, s92, 1\n"
    "s_cmp_gt_u32 s92, 0x20000\n"
    "s_cbranch_scc1 L_hv8_ok\n"
    "s_waitcnt vmcnt(0) lgkmcnt(0)\n"
    "buffer_load_dwordx4 v[160:163], v1, s[40:43], s72 offen sc1\n"
    "buffer_load_dwordx4 v[164:167], v1, s[40:43], s72 offen offset:1024 sc1\n"
    "buffer_load_dwordx4 v[168:171], v48, s[40:43], s72 offen offset:2048 sc1\n"
    "buffer_load_dwordx4 v[172:175], v44, s[40:43], s69 offen sc1\n"
    "s_waitcnt vmcnt(3)\n"
    "v_max3_u32 v51, v160, v161, v162\n"
    "v_max_u32 v51, v51, v163\n"
    "s_waitcnt vmcnt(2)\n"
    "v_max3_u32 v51, v51, v164, v165\n"
    "v_max3_u32 v51, v51, v166, v167\n"
    "s_waitcnt vmcnt(1)\n"
    "v_max3_u32 v51, v51, v168, v169\n"
    "v_max3_u32 v51, v51, v170, v171\n"
    "s_waitcnt vmcnt(0)\n"
    "v_max3_u32 v51, v51, v172, v173\n"
    "v_max3_u32 v51, v51, v174, v175\n"
    "v_cmp_eq_u32 vcc, -1, v51\n"
    "s_nop 0\n"
    "s_cbranch_vccnz L_hv8\n"
    "s_nop 7\n"
    "s_branch L_hv8_ok\n"
    "L_hv12:\n"
    "s_nop 7\n"
    "s_sleep 6\n"
    "s_add_u32 s92, s92, 1\n"
    "s_cmp_gt_u32 s92, 0x20000\n"
    "s_cbranch_scc1 L_hv12_ok\n"
    "s_waitcnt vmcnt(0) lgkmcnt(0)\n"
    "buffer_load_dwordx4 v[176:179], v44, s[40:43], s69 offen offset:1024 sc1\n"
    "buffer_load_dwordx4 v[180:183], v45, s[40:43], s69 offen sc1\n"
    "buffer_load_dwordx4 v[184:187], v45, s[40:43], s69 offen offset:1024 sc1\n"
    "buffer_load_dwordx4 v[188:191], v46, s[40:43], s69 offen sc1\n"
    "s_waitcnt vmcnt(3)\n"
    "v_max3_u32 v51, v176, v177, v178\n"
    "v_max_u32 v51, v51, v179\n"
    "s_waitcnt vmcnt(2)\n"
    "v_max3_u32 v51, v51, v180, v181\n"
    "v_max3_u32 v51, v51, v182, v183\n"
    "s_waitcnt vmcnt(1)\n"
    "v_max3_u32 v51, v51, v184, v185\n"
    "v_max3_u32 v51, v51, v186, v187\n"
    "s_waitcnt vmcnt(0)\n"
    "v_max3_u32 v51, v51, v188, v189\n"
    "v_max3_u32 v51, v51, v190, v191\n"
    "v_cmp_eq_u32 vcc, -1, v51\n"
    "s_nop 0\n"
    "s_cbranch_vccnz L_hv12\n"
    "s_nop 7\n"
    "s_branch L_hv12_ok\n"
    "L_hv16:\n"
    "s_nop 7\n"
    "s_sleep 6\n"
    "s_add_u32 s92, s92, 1\n"
    "s_cmp_gt_u32 s92, 0x20000\n"
    "s_cbranch_scc1 L_hv16_ok\n"
    "s_waitcnt vmcnt(0) lgkmcnt(0)\n"
    "buffer_load_dwordx4 v[192:195], v46, s[40:43], s69 offen offset:1024 sc1\n"
    "buffer_load_dwordx4 v[196:199], v47, s[40:43], s69 offen sc1\n"
    "buffer_load_dwordx4 v[200:203], v47, s[40:43], s69 offen offset:1024 sc1\n"
    "buffer_load_dwordx4 v[204:207], v1, s[40:43], s73 offen sc1\n"
    "s_waitcnt vmcnt(3)\n"
    "v_max3_u32 v51, v192, v193, v194\n"
    "v_max_u32 v51, v51, v195\n"
    "s_waitcnt vmcnt(2)\n"
    "v_max3_u32 v51, v51, v196, v197\n"
    "v_max3_u32 v51, v51, v198, v199\n"
    "s_waitcnt vmcnt(1)\n"
    "v_max3_u32 v51, v51, v200, v201\n"
    "v_max3_u32 v51, v51, v202, v203\n"
    "s_waitcnt vmcnt(0)\n"
    "v_max3_u32 v51, v51, v204, v205\n"
    "v_max3_u32 v51, v51, v206, v207\n"
    "v_cmp_eq_u32 vcc, -1, v51\n"
    "s_nop 0\n"
    "s_cbranch_vccnz L_hv16\n"
    "s_nop 7\n"
    "s_branch L_hv16_ok\n"
    "L_hv20:\n"
    "s_nop 7\n"
    "s_sleep 6\n"
    "s_add_u32 s92, s92, 1\n"
    "s_cmp_gt_u32 s92, 0x20000\n"
    "s_cbranch_scc1 L_hv20_ok\n"
    "s_waitcnt vmcnt(0) lgkmcnt(0)\n"
    "buffer_load_dwordx4 v[208:211], v1, s[40:43], s73 offen offset:1024 sc1\n"
    "buffer_load_dwordx4 v[212:215], v48, s[40:43], s73 offen offset:2048 sc1\n"
    "buffer_load_dwordx4 v[216:219], v44, s[40:43], s70 offen sc1\n"
    "buffer_load_dwordx4 v[220:223], v44, s[40:43], s70 offen offset:1024 sc1\n"
    "s_waitcnt vmcnt(3)\n"
    "v_max3_u32 v51, v208, v209, v210\n"
    "v_max_u32 v51, v51, v211\n"
    "s_waitcnt vmcnt(2)\n"
    "v_max3_u32 v51, v51, v212, v213\n"
    "v_max3_u32 v51, v51, v214, v215\n"
    "s_waitcnt vmcnt(1)\n"
    "v_max3_u32 v51, v51, v216, v217\n"
    "v_max3_u32 v51, v51, v218, v219\n"
    "s_waitcnt vmcnt(0)\n"
    "v_max3_u32 v51, v51, v220, v221\n"
    "v_max3_u32 v51, v51, v222, v223\n"
    "v_cmp_eq_u32 vcc, -1, v51\n"
    "s_nop 0\n"
    "s_cbranch_vccnz L_hv20\n"
    "s_nop 7\n"
    "s_branch L_hv20_ok\n"
    "L_hv24:\n"
    "s_nop 7\n"
    "s_sleep 6\n"
    "s_add_u32 s92, s92, 1\n"
    "s_cmp_gt_u32 s92, 0x20000\n"
    "s_cbranch_scc1 L_hv24_ok\n"
    "s_waitcnt vmcnt(0) lgkmcnt(0)\n"
    "buffer_load_dwordx4 v[224:227], v45, s[40:43], s70 offen sc1\n"
    "buffer_load_dwordx4 v[228:231], v45, s[40:43], s70 offen offset:1024 sc1\n"
    "buffer_load_dwordx4 v[232:235], v46, s[40:43], s70 offen sc1\n"
    "buffer_load_dwordx4 v[236:239], v46, s[40:43], s70 offen offset:1024 sc1\n"
    "s_waitcnt vmcnt(3)\n"
    "v_max3_u32 v51, v224, v225, v226\n"
    "v_max_u32 v51, v51, v227\n"
    "s_waitcnt vmcnt(2)\n"
    "v_max3_u32 v51, v51, v228, v229\n"
    "v_max3_u32 v51, v51, v230, v231\n"
    "s_waitcnt vmcnt(1)\n"
    "v_max3_u32 v51, v51, v232, v233\n"
    "v_max3_u32 v51, v51, v234, v235\n"
    "s_waitcnt vmcnt(0)\n"
    "v_max3_u32 v51, v51, v236, v237\n"
    "v_max3_u32 v51, v51, v238, v239\n"
    "v_cmp_eq_u32 vcc, -1, v51\n"
    "s_nop 0\n"
    "s_cbranch_vccnz L_hv24\n"
    "s_nop 7\n"
    "s_branch L_hv24_ok\n"
    "L_hv28:\n"
    "s_nop 7\n"
    "s_sleep 6\n"
    "s_add_u32 s92, s92, 1\n"
    "s_cmp_gt_u32 s92, 0x20000\n"
    "s_cbranch_scc1 L_hv28_ok\n"
    "s_waitcnt vmcnt(0) lgkmcnt(0)\n"
    "buffer_load_dwordx4 v[240:243], v47, s[40:43], s70 offen sc1\n"
    "buffer_load_dwordx4 v[244:247], v47, s[40:43], s70 offen offset:1024 sc1\n"
    "buffer_load_dwordx4 v[248:251], v1, s[40:43], s74 offen sc1\n"
    "buffer_load_dwordx4 v[252:255], v1, s[40:43], s74 offen offset:1024 sc1\n"
    "s_waitcnt vmcnt(3)\n"
    "v_max3_u32 v51, v240, v241, v242\n"
    "v_max_u32 v51, v51, v243\n"
    "s_waitcnt vmcnt(2)\n"
    "v_max3_u32 v51, v51, v244, v245\n"
    "v_max3_u32 v51, v51, v246, v247\n"
    "s_waitcnt vmcnt(1)\n"
    "v_max3_u32 v51, v51, v248, v249\n"
    "v_max3_u32 v51, v51, v250, v251\n"
    "s_waitcnt vmcnt(0)\n"
    "v_max3_u32 v51, v51, v252, v253\n"
    "v_max3_u32 v51, v51, v254, v255\n"
    "v_cmp_eq_u32 vcc, -1, v51\n"
    "s_nop 0\n"
    "s_cbranch_vccnz L_hv28\n"
    "s_nop 7\n"
    "s_branch L_hv28_ok\n"
    "L_hv32:\n"
    "s_nop 7\n"
    "s_sleep 6\n"
    "s_add_u32 s92, s92, 1\n"
    "s_cmp_gt_u32 s92, 0x20000\n"
    "s_cbranch_scc1 L_hv32_ok\n"
    "s_waitcnt vmcnt(0) lgkmcnt(0)\n"
    "buffer_load_dwordx4 v[128:131], v48, s[40:43], s74 offen offset:2048 sc1\n"
    "buffer_load_dwordx4 v[132:135], v44, s[40:43], s71 offen sc1\n"
    "buffer_load_dwordx4 v[136:139], v44, s[40:43], s71 offen offset:1024 sc1\n"
    "buffer_load_dwordx4 v[140:143], v45, s[40:43], s71 offen sc1\n"
    "s_waitcnt vmcnt(3)\n"
    "v_max3_u32 v51, v128, v129, v130\n"
    "v_max_u32 v51, v51, v131\n"
    "s_waitcnt vmcnt(2)\n"
    "v_max3_u32 v51, v51, v132, v133\n"
    "v_max3_u32 v51, v51, v134, v135\n"
    "s_waitcnt vmcnt(1)\n"
    "v_max3_u32 v51, v51, v136, v137\n"
    "v_max3_u32 v51, v51, v138, v139\n"
    "s_waitcnt vmcnt(0)\n"
    "v_max3_u32 v51, v51, v140, v141\n"
    "v_max3_u32 v51, v51, v142, v143\n"
    "v_cmp_eq_u32 vcc, -1, v51\n"
    "s_nop 0\n"
    "s_cbranch_vccnz L_hv32\n"
    "s_nop 7\n"
    "s_branch L_hv32_ok\n"
    "L_hv36:\n"
    "s_nop 7\n"
    "s_sleep 6\n"
    "s_add_u32 s92, s92, 1\n"
    "s_cmp_gt_u32 s92, 0x20000\n"
    "s_cbranch_scc1 L_hv36_ok\n"
    "s_waitcnt vmcnt(0) lgkmcnt(0)\n"
    "buffer_load_dwordx4 v[144:147], v45, s[40:43], s71 offen offset:1024 sc1\n"
    "buffer_load_dwordx4 v[148:151], v46, s[40:43], s71 offen sc1\n"
    "buffer_load_dwordx4 v[152:155], v46, s[40:43], s71 offen offset:1024 sc1\n"
    "buffer_load_dwordx4 v[156:159], v47, s[40:43], s71 offen sc1\n"
    "s_waitcnt vmcnt(3)\n"
    "v_max3_u32 v51, v144, v145, v146\n"
    "v_max_u32 v51, v51, v147\n"
    "s_waitcnt vmcnt(2)\n"
    "v_max3_u32 v51, v51, v148, v149\n"
    "v_max3_u32 v51, v51, v150, v151\n"
    "s_waitcnt vmcnt(1)\n"
    "v_max3_u32 v51, v51, v152, v153\n"
    "v_max3_u32 v51, v51, v154, v155\n"
    "s_waitcnt vmcnt(0)\n"
    "v_max3_u32 v51, v51, v156, v157\n"
    "v_max3_u32 v51, v51, v158, v159\n"
    "v_cmp_eq_u32 vcc, -1, v51\n"
    "s_nop 0\n"
    "s_cbranch_vccnz L_hv36\n"
    "s_nop 7\n"
    "s_branch L_hv36_ok\n"
    "L_hv40:\n"
    "s_nop 7\n"
    "s_sleep 6\n"
    "s_add_u32 s92, s92, 1\n"
    "s_cmp_gt_u32 s92, 0x20000\n"
    "s_cbranch_scc1 L_hv40_ok\n"
    "s_waitcnt vmcnt(0) lgkmcnt(0)\n"
    "buffer_load_dwordx4 v[160:163], v47, s[40:43], s71 offen offset:1024 sc1\n"
    "buffer_load_dwordx4 v[164:167], v1, s[40:43], s75 offen sc1\n"
    "buffer_load_dwordx4 v[168:171], v1, s[40:43], s75 offen offset:1024 sc1\n"
    "buffer_load_dwordx4 v[172:175], v48, s[40:43], s75 offen offset:2048 sc1\n"
    "s_waitcnt vmcnt(3)\n"
    "v_max3_u32 v51, v160, v161, v162\n"
    "v_max_u32 v51, v51, v163\n"
    "s_waitcnt vmcnt(2)\n"
    "v_max3_u32 v51, v51, v164, v165\n"
    "v_max3_u32 v51, v51, v166, v167\n"
    "s_waitcnt vmcnt(1)\n"
    "v_max3_u32 v51, v51, v168, v169\n"
    "v_max3_u32 v51, v51, v170, v171\n"
    "s_waitcnt vmcnt(0)\n"
    "v_max3_u32 v51, v51, v172, v173\n"
    "v_max3_u32 v51, v51, v174, v175\n"
    "v_cmp_eq_u32 vcc, -1, v51\n"
    "s_nop 0\n"
    "s_cbranch_vccnz L_hv40\n"
    "s_nop 7\n"
    "s_branch L_hv40_ok\n"
    "L_end:\n"

    : "+{v0}"(tid)
    : "{s[0:1]}"(__builtin_amdgcn_kernarg_segment_ptr()), "{s2}"(__builtin_amdgcn_workgroup_id_x())
    : "v1", "v2", "v3", "v4", "v5", "v6", "v7", "v8", "v9", "v10", "v11", "v12", "v13", "v14", "v15", "v16", "v17", "v18", "v19", "v20", "v21", "v22", "v23", "v24", "v25", "v26", "v27", "v28", "v29", "v30", "v31", "v32", "v33", "v34", "v35", "v36", "v37", "v38", "v39", "v40", "v41", "v42", "v43", "v44", "v45", "v46", "v47", "v48", "v49", "v50", "v51", "v52", "v53", "v54", "v55", "v56", "v57", "v58", "v59", "v60", "v61", "v62", "v63", "v64", "v65", "v66", "v67", "v68", "v69", "v70", "v71", "v72", "v73", "v74", "v75", "v76", "v77", "v78", "v79", "v80", "v81", "v82", "v83", "v84", "v85", "v86", "v87", "v88", "v89", "v90", "v91", "v92", "v93", "v94", "v95", "v96", "v97", "v98", "v99", "v100", "v101", "v102", "v103", "v104", "v105", "v106", "v107", "v108", "v109", "v110", "v111", "v112", "v113", "v114", "v115", "v116", "v117", "v118", "v119", "v120", "v121", "v122", "v123", "v124", "v125", "v126", "v127", "v128", "v129", "v130", "v131", "v132", "v133", "v134", "v135", "v136", "v137", "v138", "v139", "v140", "v141", "v142", "v143", "v144", "v145", "v146", "v147", "v148", "v149", "v150", "v151", "v152", "v153", "v154", "v155", "v156", "v157", "v158", "v159", "v160", "v161", "v162", "v163", "v164", "v165", "v166", "v167", "v168", "v169", "v170", "v171", "v172", "v173", "v174", "v175", "v176", "v177", "v178", "v179", "v180", "v181", "v182", "v183", "v184", "v185", "v186", "v187", "v188", "v189", "v190", "v191", "v192", "v193", "v194", "v195", "v196", "v197", "v198", "v199", "v200", "v201", "v202", "v203", "v204", "v205", "v206", "v207", "v208", "v209", "v210", "v211", "v212", "v213", "v214", "v215", "v216", "v217", "v218", "v219", "v220", "v221", "v222", "v223", "v224", "v225", "v226", "v227", "v228", "v229", "v230", "v231", "v232", "v233", "v234", "v235", "v236", "v237", "v238", "v239", "v240", "v241", "v242", "v243", "v244", "v245", "v246", "v247", "v248", "v249", "v250", "v251", "v252", "v253", "v254", "v255", "a0", "a1", "a2", "a3", "a4", "a5", "a6", "a7", "a8", "a9", "a10", "a11", "a12", "a13", "a14", "a15", "a16", "a17", "a18", "a19", "a20", "a21", "a22", "a23", "a24", "a25", "a26", "a27", "a28", "a29", "a30", "a31", "a32", "a33", "a34", "a35", "a36", "a37", "a38", "a39", "a40", "a41", "a42", "a43", "a44", "a45", "a46", "a47", "a48", "a49", "a50", "a51", "a52", "a53", "a54", "a55", "a56", "a57", "a58", "a59", "a60", "a61", "a62", "a63", "a64", "a65", "a66", "a67", "a68", "a69", "a70", "a71", "a72", "a73", "a74", "a75", "a76", "a77", "a78", "a79", "a80", "a81", "a82", "a83", "a84", "a85", "a86", "a87", "a88", "a89", "a90", "a91", "a92", "a93", "a94", "a95", "a96", "a97", "a98", "a99", "a100", "a101", "a102", "a103", "a104", "a105", "a106", "a107", "a108", "a109", "a110", "a111", "a112", "a113", "a114", "a115", "a116", "a117", "a118", "a119", "a120", "a121", "a122", "a123", "a124", "a125", "a126", "a127", "a128", "a129", "a130", "a131", "a132", "a133", "a134", "a135", "a136", "a137", "a138", "a139", "a140", "a141", "a142", "a143", "a144", "a145", "a146", "a147", "a148", "a149", "a150", "a151", "a152", "a153", "a154", "a155", "a156", "a157", "a158", "a159", "a160", "a161", "a162", "a163", "a164", "a165", "a166", "a167", "a168", "a169", "a170", "a171", "a172", "a173", "a174", "a175", "a176", "a177", "a178", "a179", "a180", "a181", "a182", "a183", "a184", "a185", "a186", "a187", "a188", "a189", "a190", "a191", "a192", "a193", "a194", "a195", "a196", "a197", "a198", "a199", "a200", "a201", "a202", "a203", "a204", "a205", "a206", "a207", "a208", "a209", "a210", "a211", "a212", "a213", "a214", "a215", "a216", "a217", "a218", "a219", "a220", "a221", "a222", "a223", "a224", "a225", "a226", "a227", "a228", "a229", "a230", "a231", "a232", "a233", "a234", "a235", "a236", "a237", "a238", "a239", "a240", "a241", "a242", "a243", "a244", "a245", "a246", "a247", "a248", "a249", "a250", "a251", "a252", "a253", "a254", "a255", "s3", "s4", "s5", "s6", "s7", "s8", "s9", "s10", "s11", "s12", "s13", "s14", "s15", "s16", "s17", "s18", "s19", "s20", "s21", "s22", "s23", "s24", "s25", "s26", "s27", "s28", "s29", "s30", "s31", "s32", "s33", "s34", "s35", "s36", "s37", "s38", "s39", "s40", "s41", "s42", "s43", "s44", "s45", "s46", "s47", "s48", "s49", "s50", "s51", "s52", "s53", "s54", "s55", "s56", "s57", "s58", "s59", "s60", "s61", "s62", "s63", "s64", "s65", "s66", "s67", "s68", "s69", "s70", "s71", "s72", "s73", "s74", "s75", "s76", "s77", "s78", "s79", "s80", "s81", "s82", "s83", "s84", "s85", "s86", "s87", "s88", "s89", "s90", "s91", "s92", "s93", "s94", "s95", "s96", "s97", "s98", "s99", "s100", "s101", "vcc", "scc", "memory");
#endif
}

extern "C" void kernel_launch(void* const* d_in, const int* in_sizes, int n_in, void* d_out, int out_size, void* d_ws,
                              size_t ws_size, hipStream_t stream) {
  (void)in_sizes; (void)n_in; (void)out_size; (void)ws_size;
  (void)hipFuncSetAttribute((const void*)adaln_fused, hipFuncAttributeMaxDynamicSharedMemorySize, 163840);
  const unsigned F = 0x20000u;
  adaln_fused<<<256, 256, 163840, stream>>>(d_in[0], 2048u * 1024u, F, d_in[1], 512u * 3072u, F, d_in[2], 512u * 1024u, F,
                                            d_in[4], 512u * 2752u, F, d_in[6], 1376u * 1024u, F, d_in[9], 512u * 6144u, F,
                                            d_ws, 0x1000000u, F, d_out, 2048u * 1024u, F, d_in[5], 2752u, F,
                                            d_in[7], 1024u, F, d_in[8], 1024u, F);
}
```
